```python
import torch

def _softmax(x, axis=-1):
    return torch.softmax(x, dim=axis)

def _gelu(x):
    return torch.nn.functional.gelu(x, approximate='tanh')

def _silu(x):
    return torch.nn.functional.silu(x)

def _sigmoid(x):
    return torch.sigmoid(x)

def _layernorm(x, axis=-1, eps=1e-05):
    m = x.mean(dim=axis, keepdim=True)
    v = x.var(dim=axis, keepdim=True, unbiased=False)
    return (x - m) / torch.sqrt(v + eps)

def _scatter_add(tgt, idx, src, axis=0):
    return tgt.clone().index_add_(axis, idx, src)

def _segment_sum(src, seg, n):
    out = torch.zeros((n,) + src.shape[1:], dtype=src.dtype)
    return out.index_add_(0, seg, src)

def _pad(x, pw):
    flat = tuple((v for pair in reversed(pw) for v in pair))
    return torch.nn.functional.pad(x, flat)
kernel_name = 'gated_mix__ssm_conv_s4__memory_aug_recurrent_mem_tr__prompt'

def setup_inputs():

    _w = lambda a: (lambda t: t.to(torch.bfloat16) if t.is_floating_point() else t)(torch.as_tensor(a))

    def _A_setup():
        B, L, D, N = (1, 2048, 576, 128)
        A_re = -(lambda _x: torch.exp(torch.as_tensor(_x)))((lambda *s: torch.randn(*s, dtype=torch.bfloat16) * 6.4)(D, N))
        A_im = torch.pi * (lambda *s: torch.randn(*s, dtype=torch.bfloat16) * 0.05)(D, N)
        B_re = (lambda *s: torch.randn(*s, dtype=torch.bfloat16) * 0.1)(D, N)
        B_im = (lambda *s: torch.randn(*s, dtype=torch.bfloat16) * 0.1)(D, N)
        C_re = (lambda *s: torch.randn(*s, dtype=torch.bfloat16) * 0.1)(D, N)
        C_im = (lambda *s: torch.randn(*s, dtype=torch.bfloat16) * 0.1)(D, N)
        log_dt = (lambda *s: torch.randn(*s, dtype=torch.bfloat16) * 0.05)(D) * 0.5 - 4.0
        Dskip = (lambda *s: torch.randn(*s, dtype=torch.bfloat16) * 0.05)(D)
        return {'u': _w((lambda *s: torch.randn(*s, dtype=torch.bfloat16) * 0.05)(B, L, D)), 'A_re': _w(A_re), 'A_im': _w(A_im), 'B_re': _w(B_re), 'B_im': _w(B_im), 'C_re': _w(C_re), 'C_im': _w(C_im), 'log_dt': _w(log_dt), 'Dskip': _w(Dskip)}
    _A_in = _A_setup()

    def _B_setup():
        B, S, Dm = (1, 2048, 576)
        H, Dh = (32, 128)
        M, seg = (8, 32)
        seg = min(seg, S)
        S = S // seg * seg
        return {'x': _w((lambda *s: torch.randn(*s, dtype=torch.bfloat16) * 0.05)(B, S, Dm)), 'mem0': _w((lambda *s: torch.randn(*s, dtype=torch.bfloat16) * 0.4)(B, M, Dm)), 'Wq': _w((lambda *s: torch.randn(*s, dtype=torch.bfloat16) * 6.4)(Dm, H, Dh) * Dm ** (-0.5)), 'Wk': _w((lambda *s: torch.randn(*s, dtype=torch.bfloat16) * 6.4)(Dm, H, Dh) * Dm ** (-0.5)), 'Wv': _w((lambda *s: torch.randn(*s, dtype=torch.bfloat16) * 0.4)(Dm, H, Dh) * Dm ** (-0.5)), 'Wo': _w((lambda *s: torch.randn(*s, dtype=torch.bfloat16) * 0.4)(H, Dh, Dm) * (H * Dh) ** (-0.5)), 'seg_len': seg}
    _B_in = _B_setup()
    return {'A_u': _A_in['u'], 'A_A_re': _A_in['A_re'], 'A_A_im': _A_in['A_im'], 'A_B_re': _A_in['B_re'], 'A_B_im': _A_in['B_im'], 'A_C_re': _A_in['C_re'], 'A_C_im': _A_in['C_im'], 'A_log_dt': _A_in['log_dt'], 'A_Dskip': _A_in['Dskip'], 'B_x': _B_in['x'], 'B_mem0': _B_in['mem0'], 'B_Wq': _B_in['Wq'], 'B_Wk': _B_in['Wk'], 'B_Wv': _B_in['Wv'], 'B_Wo': _B_in['Wo'], 'B_seg_len': _B_in['seg_len'], 'gate_w': _w((lambda *s: torch.randn(*s, dtype=torch.bfloat16) * 0.05)(576) * 16.0)}

def reference(A_u, A_A_re, A_A_im, A_B_re, A_B_im, A_C_re, A_C_im, A_log_dt, A_Dskip, B_x, B_mem0, B_Wq, B_Wk, B_Wv, B_Wo, B_seg_len, gate_w):

    def _A_ref(u, A_re, A_im, B_re, B_im, C_re, C_im, log_dt, Dskip):
        Bsz, L, D = u.shape
        dt = torch.exp(log_dt)[:, None]
        A = A_re + 1j * A_im
        Bc = B_re + 1j * B_im
        Cc = C_re + 1j * C_im
        logdA = dt * A
        dB = dt * Bc
        l = (lambda x, dt: x.to(dt))(torch.arange(L), A_re.dtype)
        P = torch.exp(logdA[:, :, None] * l[None, None, :])
        K = torch.einsum('dn,dnl,dn->dl', Cc, P, dB).real
        L2 = 2 * L
        Kp = _pad(K, ((0, 0), (0, L)))
        up = (lambda x, *d: x.permute(*d))(u, 0, 2, 1)
        up = _pad(up, ((0, 0), (0, 0), (0, L)))
        Kf = (lambda x, axes=(-2, -1): torch.fft.rfft2(x.float(), dim=axes))(Kp[:, None, :], axes=(-2, -1))
        Uf = (lambda x, axes=(-2, -1): torch.fft.rfft2(x.float(), dim=axes))(up[:, :, None, :], axes=(-2, -1))
        Yf = Uf * Kf[None, :, :, :]
        y = (lambda x, s, axes=(-2, -1): torch.fft.irfft2(x, s=s, dim=axes).to(torch.bfloat16))(Yf, (1, L2), axes=(-2, -1))[:, :, 0, :L]
        y = (lambda x, *d: x.permute(*d))(y, 0, 2, 1) + u * Dskip[None, None, :]
        return y

    def _B_ref(x, mem0, Wq, Wk, Wv, Wo, seg_len):
        B, S, Dm = x.shape
        H, Dh = (Wq.shape[1], Wq.shape[2])
        M = mem0.shape[1]
        scale = Dh ** (-0.5)
        n_seg = S // seg_len
        L = M + seg_len
        idx = torch.arange(L)
        mask = (idx[:, None] < M) | (idx[:, None] >= idx[None, :])
        mem = mem0
        outs = []
        for s in range(n_seg):
            xs = x[:, s * seg_len:(s + 1) * seg_len, :].clone()
            h = torch.concatenate([mem, xs], axis=1)
            q = torch.einsum('btd,dhe->bhte', h, Wq)
            k = torch.einsum('btd,dhe->bhte', h, Wk)
            v = torch.einsum('btd,dhe->bhte', h, Wv)
            scores = torch.einsum('bhqe,bhke->bhqk', q, k) * scale
            scores = torch.where(mask[None, None], scores, -1000000000.0)
            p = _softmax(scores, axis=-1)
            o = torch.einsum('bhqk,bhke->bhqe', p, v)
            o = torch.einsum('bhte,hed->btd', o, Wo)
            mem = o[:, :M, :].clone()
            outs.append(o[:, M:, :])
        return torch.concatenate(outs, axis=1)
    x = A_u
    g = _sigmoid((x * gate_w).sum(-1, keepdims=True))
    a = _A_ref(x, A_A_re, A_A_im, A_B_re, A_B_im, A_C_re, A_C_im, A_log_dt, A_Dskip)
    b = _B_ref(x, B_mem0, B_Wq, B_Wk, B_Wv, B_Wo, B_seg_len)
    return g * a + (1 - g) * b
```

```cpp
#include <hip/hip_runtime.h>
#include <hip/hip_bf16.h>
typedef unsigned short u16;
__device__ __forceinline__ float bf2f(u16 b) { return __uint_as_float(((unsigned)b) << 16); }
__device__ __forceinline__ u16 f2bf(float f) { unsigned u = __float_as_uint(f); return (u16)((u + 0x7fffu + ((u >> 16) & 1u)) >> 16); }
__device__ __forceinline__ float rbf(float f) { return bf2f(f2bf(f)); }
constexpr int L = 2048, D = 576, N = 128, H = 32, DH = 128, M = 8, SEG = 32, NSEG = 64, HD = H * DH;

__global__ void kgen(const u16* Are, const u16* Aim, const u16* Bre, const u16* Bim, const u16* Cre, const u16* Cim, const u16* logdt, float* K) {
    int d = blockIdx.y, l = blockIdx.x * blockDim.x + threadIdx.x;
    float dt = rbf(expf(bf2f(logdt[d])));
    float lf = rbf((float)l);
    float acc = 0.f;
    for (int n = 0; n < N; ++n) {
        float ar = dt * bf2f(Are[d * N + n]), ai = dt * bf2f(Aim[d * N + n]);
        float br = dt * bf2f(Bre[d * N + n]), bi = dt * bf2f(Bim[d * N + n]);
        float cr = bf2f(Cre[d * N + n]), ci = bf2f(Cim[d * N + n]);
        float xr = cr * br - ci * bi, xi = cr * bi + ci * br;
        float e = expf(ar * lf), s, c; sincosf(ai * lf, &s, &c);
        acc += e * (xr * c - xi * s);
    }
    K[d * L + l] = acc;
}
__global__ void conv(const u16* u, const float* K, const u16* Dskip, u16* a) {
    int d = blockIdx.y, t = blockIdx.x * blockDim.x + threadIdx.x;
    float acc = 0.f;
    for (int j = 0; j <= t; ++j) acc += K[d * L + j] * bf2f(u[(t - j) * D + d]);
    float y = rbf(acc);
    float sk = rbf(bf2f(u[t * D + d]) * bf2f(Dskip[d]));
    a[t * D + d] = f2bf(y + sk);
}
__global__ void gemm(const u16* x, const u16* W, u16* out, int K, int NC) {
    int r = blockIdx.y, c = blockIdx.x * blockDim.x + threadIdx.x;
    if (c >= NC) return;
    float acc = 0.f;
    for (int k = 0; k < K; ++k) acc += bf2f(x[r * K + k]) * bf2f(W[(size_t)k * NC + c]);
    out[(size_t)r * NC + c] = f2bf(acc);
}
__global__ void attn(const u16* qm, const u16* km, const u16* vm, const u16* qx, const u16* kx, const u16* vx, u16* om, u16* ox, int seg) {
    int row = blockIdx.x, h = blockIdx.y, t = threadIdx.x;
    __shared__ float p[40];
    const u16* q = row < M ? qm + row * HD + h * DH : qx + (size_t)(seg * SEG + row - M) * HD + h * DH;
    if (t < 40) {
        const u16* k = t < M ? km + t * HD + h * DH : kx + (size_t)(seg * SEG + t - M) * HD + h * DH;
        float acc = 0.f;
        for (int e = 0; e < DH; ++e) acc += bf2f(q[e]) * bf2f(k[e]);
        float s = rbf(rbf(acc) * 0.08838834764831845f);
        if (!(row < M || row >= t)) s = rbf(-1e9f);
        p[t] = s;
    }
    __syncthreads();
    float mx = -INFINITY; for (int j = 0; j < 40; ++j) mx = fmaxf(mx, p[j]);
    float sum = 0.f; for (int j = 0; j < 40; ++j) sum += expf(p[j] - mx);
    for (int e = t; e < DH; e += 64) {
        float acc = 0.f;
        for (int j = 0; j < 40; ++j) {
            const u16* v = j < M ? vm + j * HD + h * DH : vx + (size_t)(seg * SEG + j - M) * HD + h * DH;
            acc += rbf(expf(p[j] - mx) / sum) * bf2f(v[e]);
        }
        if (row < M) om[row * HD + h * DH + e] = f2bf(acc); else ox[(size_t)(seg * SEG + row - M) * HD + h * DH + e] = f2bf(acc);
    }
}
__global__ void combine(const u16* u, const u16* gw, const u16* a, const u16* b, u16* out) {
    int t = blockIdx.x; __shared__ float red[64];
    float acc = 0.f;
    for (int d = threadIdx.x; d < D; d += 64) acc += rbf(bf2f(u[t * D + d]) * bf2f(gw[d]));
    red[threadIdx.x] = acc; __syncthreads();
    float s = 0.f; for (int i = 0; i < 64; ++i) s += red[i];
    s = rbf(s);
    float g = rbf(1.f / (1.f + expf(-s)));
    float og = rbf(1.f - g);
    for (int d = threadIdx.x; d < D; d += 64) out[t * D + d] = f2bf(rbf(g * bf2f(a[t * D + d])) + rbf(og * bf2f(b[t * D + d])));
}
extern "C" void kernel_launch(void* const* d_in, const int* in_sizes, int n_in, void* d_out, int out_size, void* d_ws, size_t ws_size, hipStream_t stream) {
    const u16* u = (const u16*)d_in[0];
    const u16 *Are = (const u16*)d_in[1], *Aim = (const u16*)d_in[2], *Bre = (const u16*)d_in[3], *Bim = (const u16*)d_in[4], *Cre = (const u16*)d_in[5], *Cim = (const u16*)d_in[6];
    const u16 *logdt = (const u16*)d_in[7], *Dskip = (const u16*)d_in[8], *mem0 = (const u16*)d_in[10];
    const u16 *Wq = (const u16*)d_in[11], *Wk = (const u16*)d_in[12], *Wv = (const u16*)d_in[13], *Wo = (const u16*)d_in[14], *gw = (const u16*)d_in[16];
    char* ws = (char*)d_ws;
    float* K = (float*)ws; ws += (size_t)D * L * 4;
    u16* a = (u16*)ws; ws += (size_t)L * D * 2;
    u16* b = (u16*)ws; ws += (size_t)L * D * 2;
    u16* qx = (u16*)ws; ws += (size_t)L * HD * 2;
    u16* kx = (u16*)ws; ws += (size_t)L * HD * 2;
    u16* vx = (u16*)ws; ws += (size_t)L * HD * 2;
    u16* ox = (u16*)ws; ws += (size_t)L * HD * 2;
    u16* qm = (u16*)ws; ws += M * HD * 2; u16* km = (u16*)ws; ws += M * HD * 2; u16* vm = (u16*)ws; ws += M * HD * 2; u16* om = (u16*)ws; ws += M * HD * 2;
    u16* mem = (u16*)ws; ws += M * D * 2;
    kgen<<<dim3(L / 256, D), 256, 0, stream>>>(Are, Aim, Bre, Bim, Cre, Cim, logdt, K);
    conv<<<dim3(L / 256, D), 256, 0, stream>>>(u, K, Dskip, a);
    gemm<<<dim3(HD / 256, L), 256, 0, stream>>>(u, Wq, qx, D, HD);
    gemm<<<dim3(HD / 256, L), 256, 0, stream>>>(u, Wk, kx, D, HD);
    gemm<<<dim3(HD / 256, L), 256, 0, stream>>>(u, Wv, vx, D, HD);
    for (int s = 0; s < NSEG; ++s) {
        const u16* mc = s == 0 ? mem0 : mem;
        gemm<<<dim3(HD / 256, M), 256, 0, stream>>>(mc, Wq, qm, D, HD);
        gemm<<<dim3(HD / 256, M), 256, 0, stream>>>(mc, Wk, km, D, HD);
        gemm<<<dim3(HD / 256, M), 256, 0, stream>>>(mc, Wv, vm, D, HD);
        attn<<<dim3(40, H), 64, 0, stream>>>(qm, km, vm, qx, kx, vx, om, ox, s);
        gemm<<<dim3(3, M), 256, 0, stream>>>(om, Wo, mem, HD, D);
    }
    gemm<<<dim3(3, L), 256, 0, stream>>>(ox, Wo, b, HD, D);
    combine<<<L, 64, 0, stream>>>(u, gw, a, b, (u16*)d_out);
}
```

```cpp
#include <hip/hip_runtime.h>
#include <hip/hip_bf16.h>
typedef unsigned short u16;
typedef unsigned u32;
typedef unsigned long long u64;
#define GAS __attribute__((address_space(1)))
#define LAS __attribute__((address_space(3)))
typedef GAS u32 gu32;
typedef u32 u32x2 __attribute__((ext_vector_type(2)));
typedef u32 u32x4 __attribute__((ext_vector_type(4)));
typedef u32 u32x8 __attribute__((ext_vector_type(8)));
typedef float f32x4 __attribute__((ext_vector_type(4)));
typedef float f32x16 __attribute__((ext_vector_type(16)));
typedef __bf16 bf16x8 __attribute__((ext_vector_type(8)));
typedef __bf16 bf16x16 __attribute__((ext_vector_type(16)));
typedef __bf16 bf16x2 __attribute__((ext_vector_type(2)));
typedef short s16x4 __attribute__((ext_vector_type(4)));
typedef __amdgpu_buffer_rsrc_t rsrc_t;
#define RLX_AGENT __ATOMIC_RELAXED, __HIP_MEMORY_SCOPE_AGENT
#define FENCE() asm volatile("" ::: "memory")
#define SB() __builtin_amdgcn_sched_barrier(0)
#define UNI(x) ((u32)__builtin_amdgcn_readfirstlane((int)(x)))
#define BAR() { asm volatile("s_waitcnt lgkmcnt(0)\n\ts_barrier" ::: "memory"); SB(); }
#define VMWAIT(n) asm volatile("s_waitcnt vmcnt(" #n ")" ::: "memory")
#define OOB 0x80000000u

__device__ __forceinline__ float bf2f(u16 b) { return __uint_as_float(((unsigned)b) << 16); }
__device__ __forceinline__ u32 pk2(float lo, float hi) { bf16x2 v; v[0] = (__bf16)lo; v[1] = (__bf16)hi; return __builtin_bit_cast(u32, v); }
__device__ __forceinline__ float rbf(float f) { return __uint_as_float(pk2(f, 0.f) << 16); }
__device__ __forceinline__ float lo_f(u32 p) { return __uint_as_float(p << 16); }
__device__ __forceinline__ float hi_f(u32 p) { return __uint_as_float(p & 0xffff0000u); }
constexpr int L = 2048, D = 576, NM = 128, H = 32, DH = 128, M = 8, SEG = 32, NSEG = 64, HD = H * DH;

constexpr u32 WS_CTL = 0;
constexpr u32 WS_COMM = 65536;
constexpr int KB_MEM = 192, KB_MEM8 = 216, KB_FA = 224;
#define BIGOFF 2
constexpr u32 WS_KXF = (u32)BIGOFF << 20;
constexpr u32 WS_VXF = WS_KXF + (16u << 20);
constexpr u32 WS_QXF = WS_VXF + (16u << 20);
constexpr u32 WS_OXF = WS_QXF + (16u << 20);
constexpr u32 WS_KM = WS_OXF + (16u << 20);
constexpr u32 WS_VMT = WS_KM + (4u << 20);
constexpr u32 WS_YA = WS_VMT + (4u << 20);
constexpr u32 WS_G = WS_YA + (4u << 20);
constexpr int C_PHASE = 0, C_EPOCH = 32, C_TMO = 64, C_ORDY = 128, C_SLOT = 512, C_KMF = 1024, C_KVF = 2048, C_QF = 4096, C_CF = 8192, C_GF = 8448;
__host__ __device__ constexpr u32 kbo(int k) { return (u32)((k >> 3) * 16384 + (k & 7) * 1024); }
__host__ __device__ constexpr u32 lineoff(int kb0, int line) { return kbo(kb0 + (line >> 3)) + (line & 7) * 128; }
constexpr u32 WS_OF = 1u << 20;
constexpr int SPIN_CAP = 1 << 18;

__device__ __forceinline__ u32x4 bld16(rsrc_t r, u32 voff, u32 soff) { return __builtin_amdgcn_raw_buffer_load_b128(r, voff, soff, 16); }
__device__ __forceinline__ u32x2 bld8(rsrc_t r, u32 voff, u32 soff) { return __builtin_amdgcn_raw_buffer_load_b64(r, voff, soff, 16); }
__device__ __forceinline__ u32 bld4(rsrc_t r, u32 voff, u32 soff) { return __builtin_amdgcn_raw_buffer_load_b32(r, voff, soff, 16); }
__device__ __forceinline__ void bst16(rsrc_t r, u32 voff, u32 soff, u32x4 v) { __builtin_amdgcn_raw_buffer_store_b128(v, r, voff, soff, 16); }
__device__ __forceinline__ void bst8(rsrc_t r, u32 voff, u32 soff, u32x2 v) { __builtin_amdgcn_raw_buffer_store_b64(v, r, voff, soff, 16); }
__device__ __forceinline__ void bst4(rsrc_t r, u32 voff, u32 soff, u32 v) { __builtin_amdgcn_raw_buffer_store_b32(v, r, voff, soff, 16); }
__device__ __forceinline__ void dma16(const void* src, LAS char* dst) {
    u32 keep; const u32 d = UNI((u32)(size_t)dst);
    asm volatile("s_mov_b32 %0, m0\n\ts_mov_b32 m0, %2\n\ts_nop 0\n\tglobal_load_lds_dwordx4 %1, off\n\ts_mov_b32 m0, %0" : "=&s"(keep) : "v"(src), "s"(d) : "memory");
}
__device__ __forceinline__ u32x2 trread(const LAS char* p) { return __builtin_bit_cast(u32x2, __builtin_amdgcn_ds_read_tr16_b64_v4i16((LAS s16x4*)p)); }

struct Args {
    const u16 *u, *Are, *Aim, *Bre, *Bim, *Cre, *Cim, *logdt, *Dskip, *mem0, *Wq, *Wk, *Wv, *Wo, *gw;
    u16* out;
    char* ws;
};

constexpr int LDS_MEM = 0;
constexpr int LDS_Q = LDS_MEM + 9216;
constexpr int LDS_K = LDS_Q + 2048;
constexpr int LDS_KX = LDS_K + 2048;
constexpr int LDS_W = LDS_KX + 8192;
constexpr int LDS_STG = 13312;
constexpr int LDS_ROLE = LDS_STG + 3 * 49152;
constexpr int LDS_FLG = 163776;
constexpr int LDS_TOTAL = 160 * 1024;
static_assert(LDS_ROLE + 16 <= LDS_TOTAL && LDS_W + 48 * 2048 <= LDS_ROLE, "lds");

__device__ __forceinline__ f32x4 mf32(u32x4 a, u32x4 b, f32x4 c) {
    return __builtin_amdgcn_mfma_f32_16x16x32_bf16(__builtin_bit_cast(bf16x8, a), __builtin_bit_cast(bf16x8, b), c, 0, 0, 0);
}
__device__ __forceinline__ f32x4 mf16(u32x2 a, u32x2 b, f32x4 c) {
    return __builtin_amdgcn_mfma_f32_16x16x16bf16_1k(__builtin_bit_cast(s16x4, a), __builtin_bit_cast(s16x4, b), c, 0, 0, 0);
}
__device__ __forceinline__ f32x16 mf3232(u32x4 a, u32x4 b, f32x16 c) {
    return __builtin_amdgcn_mfma_f32_32x32x16_bf16(__builtin_bit_cast(bf16x8, a), __builtin_bit_cast(bf16x8, b), c, 0, 0, 0);
}
__device__ __forceinline__ float swapadd32(float a, float b) {
    auto r = __builtin_amdgcn_permlane32_swap(__float_as_uint(a), __float_as_uint(b), false, false);
    return __uint_as_float(r[0]) + __uint_as_float(r[1]);
}
__device__ __forceinline__ float swapadd16(float a, float b) {
    auto r = __builtin_amdgcn_permlane16_swap(__float_as_uint(a), __float_as_uint(b), false, false);
    return __uint_as_float(r[0]) + __uint_as_float(r[1]);
}
__device__ __forceinline__ float xmax16(float a) { auto r = __builtin_amdgcn_permlane16_swap(__float_as_uint(a), __float_as_uint(a), false, false); return fmaxf(__uint_as_float(r[0]), __uint_as_float(r[1])); }
__device__ __forceinline__ float xmax32(float a) { auto r = __builtin_amdgcn_permlane32_swap(__float_as_uint(a), __float_as_uint(a), false, false); return fmaxf(__uint_as_float(r[0]), __uint_as_float(r[1])); }
__device__ __forceinline__ u32x8 toV(u32x8 v) { asm volatile("" : "+v"(v)); return v; }
__device__ __forceinline__ u32x4 toA4(u32x4 v) { u32x4 r; asm volatile("" : "=a"(r) : "0"(v)); return r; }
__device__ __forceinline__ u32x4 toV4(u32x4 v) { asm volatile("" : "+v"(v)); return v; }
__device__ __forceinline__ u64 clk() { u64 t; SB(); asm volatile("s_memtime %0\n\ts_waitcnt lgkmcnt(0)" : "=s"(t) :: "memory"); SB(); return t; }
__device__ __forceinline__ void set_tmo(gu32* ctl, u32 ep) { if ((threadIdx.x & 63) == 0) __hip_atomic_store(ctl + C_TMO, ep, RLX_AGENT); }
__device__ __forceinline__ int wait_phase(gu32* ctl, u32 ep) {
    u32 phase; int sp = 0;
    do { phase = UNI(__hip_atomic_load(ctl + C_PHASE, RLX_AGENT)) - ep; } while (phase > 1u && ++sp < SPIN_CAP * 4);
    return phase & 1;
}
__device__ __forceinline__ void wait_flags(rsrc_t RW, u32 voff, u32 ep, u32 need, gu32* ctl, bool& dead) {
    if (dead) return;
    for (int sp = 0;; ++sp) {
        const u32 f = bld4(RW, voff, 0);
        FENCE();
        if (!__any(voff != OOB && f - ep - need > (need ? 64u - need : 0u))) break;
        __builtin_amdgcn_s_sleep(2);
        if (sp > SPIN_CAP || ((sp & 255) == 255 && UNI(__hip_atomic_load(ctl + C_TMO, RLX_AGENT)) == ep)) { dead = true; set_tmo(ctl, ep); break; }
    }
}

__device__ __forceinline__ void head_role(const Args& a, int h, LAS char* lds, u32 ep) {
    const int tid = threadIdx.x, w = __builtin_amdgcn_readfirstlane(tid >> 6), l = tid & 63, cl = l & 15, g = l >> 4;
    char* ws = a.ws;
    gu32* ctl = (gu32*)(ws + WS_CTL);
    {
        const u16* mp = a.mem0 + (4 * (cl >> 3) + (cl & 3)) * D + (2 * g + ((cl >> 2) & 1)) * 8;
        dma16(mp + w * 64, lds + LDS_MEM + w * 1024); dma16(mp + (w + 4) * 64, lds + LDS_MEM + (w + 4) * 1024);
        if (w == 0) dma16(mp + 512, lds + LDS_MEM + 8192);
    }
    if (tid < 4) *(LAS u32*)(lds + LDS_FLG + tid * 4) = 0u;
    const u64 wb_ = (u64)ws;
    const u32x4 RW = {(u32)wb_, (u32)(wb_ >> 32), 0x7fffffffu, 0x00020000u};
    const u64 W0 = (u64)(a.Wk + h * DH), W1 = (u64)(a.Wv + h * DH), W2 = (u64)(a.Wq + h * DH), comm = (u64)(ws + WS_COMM);
    const int idx = ((cl >> 2) & 1) ? 0xEEEE : 0x4444;
    const u32 l16 = l * 16;
    const u32 wl = LDS_W + w * (12 * 2048) + l * 16;
    const int tkw = 4 * (g >> 1) + 2 * (g & 1);
    const int octw = 4 * w + (cl >> 2);
    const u32 qw = LDS_Q + octw * 128 + ((tkw ^ (4 * (octw & 1))) * 16) + (cl & 3) * 4, kw = qw + 2048;
    const u32 qrd = LDS_Q + g * 128 + (((cl & 7) ^ (4 * (g & 1))) * 16);
    const u32 krd = LDS_K + g * 128 + (((4 * (cl >> 3) + (cl & 3)) ^ (4 * (g & 1))) * 16);
    const u32 ooff = cl >= 8 ? OOB : kbo(((cl >> 2) & 1) * 32 + h) + ((2 * (w & 1) + (g >> 1)) * 16 + (w >> 1) * 8 + (g & 1) * 4 + (cl & 3)) * 16;
    const u32 vmoff = (32 * w + 2 * cl + (g & 1)) * 16 + (g >> 1) * 8;
    const u32 kmoff = (cl & 4) ? OOB : g * 128 + (4 * (cl >> 3) + (cl & 3)) * 16;
    const u32 mhi8 = cl >= 8 ? ~0u : 0u, mg1 = (g & 1) ? ~0u : 0u;
    const u32 mw0 = LDS_MEM + w * 1024 + l16, mw2s = UNI(LDS_MEM + 8192 + w * 256);
    const u32 kxw = UNI(LDS_KX + w * 2048), flw = UNI(LDS_FLG + w * 4);
    const u32 kx0 = UNI(WS_KXF + h * 8192 + w * 2048), vx0 = UNI(WS_VXF + h * 8192 + w * 2048), km0 = UNI(WS_KM + h * 2048 + w * 512), vm0 = UNI(WS_VMT + h * 2048);
    const u32 mp0 = UNI(kbo(KB_MEM) + w * 1024), mp2 = UNI(kbo(KB_MEM8) + w * 256);
    const u32 fas = UNI(w == 0 ? lineoff(KB_FA, h) : OOB), kmfs = UNI(w == 0 ? (u32)(WS_CTL + (C_KMF + h * 32) * 4) : OOB), kvfs = UNI(WS_CTL + (C_KVF + h) * 4);
    const u32 fboff = lineoff(KB_FA, l & 31);
    const u32 ords = WS_CTL + C_ORDY * 4;
    u32 dead;
    asm volatile(
        "v_lshrrev_b32 v104, 8, %[l16]\n\t"
        "v_bfe_u32 v105, %[l16], 4, 4\n\t"
        "v_and_b32 v106, 1, v104\n\t"
        "v_lshlrev_b32 v106, 1, v106\n\t"
        "v_xor_b32 v106, v106, v105\n\t"
        "v_xor_b32 v107, 0, v106\n\t"
        "v_lshlrev_b32 v107, 4, v107\n\t"
        "v_lshl_add_u32 v96, v104, 13, v107\n\t"
        "v_xor_b32 v107, 4, v106\n\t"
        "v_lshlrev_b32 v107, 4, v107\n\t"
        "v_lshl_add_u32 v97, v104, 13, v107\n\t"
        "v_xor_b32 v107, 8, v106\n\t"
        "v_lshlrev_b32 v107, 4, v107\n\t"
        "v_lshl_add_u32 v98, v104, 13, v107\n\t"
        "v_xor_b32 v107, 12, v106\n\t"
        "v_lshlrev_b32 v107, 4, v107\n\t"
        "v_lshl_add_u32 v99, v104, 13, v107\n\t"
        "v_lshrrev_b32 v106, 1, v104\n\t"
        "v_lshlrev_b32 v106, 4, v106\n\t"
        "v_and_b32 v107, 8, v105\n\t"
        "v_or_b32 v106, v106, v107\n\t"
        "v_and_b32 v107, 1, v104\n\t"
        "v_lshl_or_b32 v106, v107, 2, v106\n\t"
        "v_bfe_u32 v107, v105, 2, 1\n\t"
        "v_or_b32 v106, v106, v107\n\t"
        "v_and_b32 v108, 12, v106\n\t"
        "v_lshl_or_b32 v108, v107, 1, v108\n\t"
        "v_bfe_u32 v107, v105, 1, 1\n\t"
        "s_lshl_b32 s64, %[w], 2\n\t"
        "v_or_b32 v107, s64, v107\n\t"
        "v_xor_b32 v107, v107, v108\n\t"
        "v_lshlrev_b32 v107, 4, v107\n\t"
        "v_lshl_or_b32 v107, v106, 8, v107\n\t"
        "v_and_b32 v108, 1, v105\n\t"
        "v_lshl_or_b32 v107, v108, 3, v107\n\t"
        "s_lshl_b32 s64, %[w], 14\n\t"
        "v_or_b32 v100, s64, v107\n\t"
        "v_xor_b32 v101, 32, v100\n\t"
        "s_mov_b32 s58, 0x7fffffff\n\t"
        "s_mov_b32 s59, 0x00020000\n\t"
        "s_mov_b64 s[56:57], %[W0]\n\t"
        "s_mul_i32 s64, %[w], 524288\n\t"
        "s_add_u32 s64, s64, 0\n\t"
        "s_mul_i32 s65, %[w], 16384\n\t"
        "s_add_u32 s65, s65, 13312\n\t"
        "s_add_u32 m0, s65, 0\n\t"
        "s_nop 0\n\t"
        "buffer_load_dwordx4 v96, s[56:59], s64 offen lds\n\t"
        "s_add_u32 m0, s65, 1024\n\t"
        "s_add_u32 s64, s64, 32768\n\t"
        "buffer_load_dwordx4 v97, s[56:59], s64 offen lds\n\t"
        "s_add_u32 m0, s65, 2048\n\t"
        "s_add_u32 s64, s64, 32768\n\t"
        "buffer_load_dwordx4 v98, s[56:59], s64 offen lds\n\t"
        "s_add_u32 m0, s65, 3072\n\t"
        "s_add_u32 s64, s64, 32768\n\t"
        "buffer_load_dwordx4 v99, s[56:59], s64 offen lds\n\t"
        "s_add_u32 m0, s65, 4096\n\t"
        "s_add_u32 s64, s64, 32768\n\t"
        "buffer_load_dwordx4 v96, s[56:59], s64 offen lds\n\t"
        "s_add_u32 m0, s65, 5120\n\t"
        "s_add_u32 s64, s64, 32768\n\t"
        "buffer_load_dwordx4 v97, s[56:59], s64 offen lds\n\t"
        "s_add_u32 m0, s65, 6144\n\t"
        "s_add_u32 s64, s64, 32768\n\t"
        "buffer_load_dwordx4 v98, s[56:59], s64 offen lds\n\t"
        "s_add_u32 m0, s65, 7168\n\t"
        "s_add_u32 s64, s64, 32768\n\t"
        "buffer_load_dwordx4 v99, s[56:59], s64 offen lds\n\t"
        "s_add_u32 m0, s65, 8192\n\t"
        "s_add_u32 s64, s64, 32768\n\t"
        "buffer_load_dwordx4 v96, s[56:59], s64 offen lds\n\t"
        "s_add_u32 m0, s65, 9216\n\t"
        "s_add_u32 s64, s64, 32768\n\t"
        "buffer_load_dwordx4 v97, s[56:59], s64 offen lds\n\t"
        "s_add_u32 m0, s65, 10240\n\t"
        "s_add_u32 s64, s64, 32768\n\t"
        "buffer_load_dwordx4 v98, s[56:59], s64 offen lds\n\t"
        "s_add_u32 m0, s65, 11264\n\t"
        "s_add_u32 s64, s64, 32768\n\t"
        "buffer_load_dwordx4 v99, s[56:59], s64 offen lds\n\t"
        "s_add_u32 m0, s65, 12288\n\t"
        "s_add_u32 s64, s64, 32768\n\t"
        "buffer_load_dwordx4 v96, s[56:59], s64 offen lds\n\t"
        "s_add_u32 m0, s65, 13312\n\t"
        "s_add_u32 s64, s64, 32768\n\t"
        "buffer_load_dwordx4 v97, s[56:59], s64 offen lds\n\t"
        "s_add_u32 m0, s65, 14336\n\t"
        "s_add_u32 s64, s64, 32768\n\t"
        "buffer_load_dwordx4 v98, s[56:59], s64 offen lds\n\t"
        "s_add_u32 m0, s65, 15360\n\t"
        "s_add_u32 s64, s64, 32768\n\t"
        "buffer_load_dwordx4 v99, s[56:59], s64 offen lds\n\t"
        "s_mov_b64 s[56:57], %[W0]\n\t"
        "s_mul_i32 s64, %[w], 524288\n\t"
        "s_add_u32 s64, s64, 2097152\n\t"
        "s_mul_i32 s65, %[w], 16384\n\t"
        "s_add_u32 s65, s65, 78848\n\t"
        "s_add_u32 m0, s65, 0\n\t"
        "s_nop 0\n\t"
        "buffer_load_dwordx4 v96, s[56:59], s64 offen lds\n\t"
        "s_add_u32 m0, s65, 1024\n\t"
        "s_add_u32 s64, s64, 32768\n\t"
        "buffer_load_dwordx4 v97, s[56:59], s64 offen lds\n\t"
        "s_add_u32 m0, s65, 2048\n\t"
        "s_add_u32 s64, s64, 32768\n\t"
        "buffer_load_dwordx4 v98, s[56:59], s64 offen lds\n\t"
        "s_add_u32 m0, s65, 3072\n\t"
        "s_add_u32 s64, s64, 32768\n\t"
        "buffer_load_dwordx4 v99, s[56:59], s64 offen lds\n\t"
        "s_add_u32 m0, s65, 4096\n\t"
        "s_add_u32 s64, s64, 32768\n\t"
        "buffer_load_dwordx4 v96, s[56:59], s64 offen lds\n\t"
        "s_add_u32 m0, s65, 5120\n\t"
        "s_add_u32 s64, s64, 32768\n\t"
        "buffer_load_dwordx4 v97, s[56:59], s64 offen lds\n\t"
        "s_add_u32 m0, s65, 6144\n\t"
        "s_add_u32 s64, s64, 32768\n\t"
        "buffer_load_dwordx4 v98, s[56:59], s64 offen lds\n\t"
        "s_add_u32 m0, s65, 7168\n\t"
        "s_add_u32 s64, s64, 32768\n\t"
        "buffer_load_dwordx4 v99, s[56:59], s64 offen lds\n\t"
        "s_add_u32 m0, s65, 8192\n\t"
        "s_add_u32 s64, s64, 32768\n\t"
        "buffer_load_dwordx4 v96, s[56:59], s64 offen lds\n\t"
        "s_add_u32 m0, s65, 9216\n\t"
        "s_add_u32 s64, s64, 32768\n\t"
        "buffer_load_dwordx4 v97, s[56:59], s64 offen lds\n\t"
        "s_add_u32 m0, s65, 10240\n\t"
        "s_add_u32 s64, s64, 32768\n\t"
        "buffer_load_dwordx4 v98, s[56:59], s64 offen lds\n\t"
        "s_add_u32 m0, s65, 11264\n\t"
        "s_add_u32 s64, s64, 32768\n\t"
        "buffer_load_dwordx4 v99, s[56:59], s64 offen lds\n\t"
        "s_add_u32 m0, s65, 12288\n\t"
        "s_add_u32 s64, s64, 32768\n\t"
        "buffer_load_dwordx4 v96, s[56:59], s64 offen lds\n\t"
        "s_add_u32 m0, s65, 13312\n\t"
        "s_add_u32 s64, s64, 32768\n\t"
        "buffer_load_dwordx4 v97, s[56:59], s64 offen lds\n\t"
        "s_add_u32 m0, s65, 14336\n\t"
        "s_add_u32 s64, s64, 32768\n\t"
        "buffer_load_dwordx4 v98, s[56:59], s64 offen lds\n\t"
        "s_add_u32 m0, s65, 15360\n\t"
        "s_add_u32 s64, s64, 32768\n\t"
        "buffer_load_dwordx4 v99, s[56:59], s64 offen lds\n\t"
        "s_waitcnt vmcnt(16) lgkmcnt(0)\n\t"
        "s_barrier\n\t"
        "s_mov_b64 s[56:57], %[W0]\n\t"
        "s_mul_i32 s64, %[w], 131072\n\t"
        "s_add_u32 s64, s64, 4194304\n\t"
        "s_mul_i32 s65, %[w], 4096\n\t"
        "s_add_u32 s65, s65, 144384\n\t"
        "s_add_u32 m0, s65, 0\n\t"
        "s_nop 0\n\t"
        "buffer_load_dwordx4 v96, s[56:59], s64 offen lds\n\t"
        "s_add_u32 m0, s65, 1024\n\t"
        "s_add_u32 s64, s64, 32768\n\t"
        "buffer_load_dwordx4 v97, s[56:59], s64 offen lds\n\t"
        "s_add_u32 m0, s65, 2048\n\t"
        "s_add_u32 s64, s64, 32768\n\t"
        "buffer_load_dwordx4 v98, s[56:59], s64 offen lds\n\t"
        "s_add_u32 m0, s65, 3072\n\t"
        "s_add_u32 s64, s64, 32768\n\t"
        "buffer_load_dwordx4 v99, s[56:59], s64 offen lds\n\t"
        "v_xor_b32 v102, 0, v100\n\t"
        "ds_read_b64_tr_b16 a[0:1], v102 offset:13312\n\t"
        "ds_read_b64_tr_b16 a[2:3], v102 offset:13824\n\t"
        "ds_read_b64_tr_b16 a[4:5], v102 offset:21504\n\t"
        "ds_read_b64_tr_b16 a[6:7], v102 offset:22016\n\t"
        "v_xor_b32 v102, 0, v101\n\t"
        "ds_read_b64_tr_b16 a[72:73], v102 offset:13312\n\t"
        "ds_read_b64_tr_b16 a[74:75], v102 offset:13824\n\t"
        "ds_read_b64_tr_b16 a[76:77], v102 offset:21504\n\t"
        "ds_read_b64_tr_b16 a[78:79], v102 offset:22016\n\t"
        "v_xor_b32 v102, 16384, v100\n\t"
        "ds_read_b64_tr_b16 a[16:17], v102 offset:13312\n\t"
        "ds_read_b64_tr_b16 a[18:19], v102 offset:13824\n\t"
        "ds_read_b64_tr_b16 a[20:21], v102 offset:21504\n\t"
        "ds_read_b64_tr_b16 a[22:23], v102 offset:22016\n\t"
        "v_xor_b32 v102, 16384, v101\n\t"
        "ds_read_b64_tr_b16 a[88:89], v102 offset:13312\n\t"
        "ds_read_b64_tr_b16 a[90:91], v102 offset:13824\n\t"
        "ds_read_b64_tr_b16 a[92:93], v102 offset:21504\n\t"
        "ds_read_b64_tr_b16 a[94:95], v102 offset:22016\n\t"
        "v_xor_b32 v102, 32768, v100\n\t"
        "ds_read_b64_tr_b16 a[32:33], v102 offset:13312\n\t"
        "ds_read_b64_tr_b16 a[34:35], v102 offset:13824\n\t"
        "ds_read_b64_tr_b16 a[36:37], v102 offset:21504\n\t"
        "ds_read_b64_tr_b16 a[38:39], v102 offset:22016\n\t"
        "v_xor_b32 v102, 32768, v101\n\t"
        "ds_read_b64_tr_b16 a[104:105], v102 offset:13312\n\t"
        "ds_read_b64_tr_b16 a[106:107], v102 offset:13824\n\t"
        "ds_read_b64_tr_b16 a[108:109], v102 offset:21504\n\t"
        "ds_read_b64_tr_b16 a[110:111], v102 offset:22016\n\t"
        "v_xor_b32 v102, 49152, v100\n\t"
        "ds_read_b64_tr_b16 a[48:49], v102 offset:13312\n\t"
        "ds_read_b64_tr_b16 a[50:51], v102 offset:13824\n\t"
        "ds_read_b64_tr_b16 a[52:53], v102 offset:21504\n\t"
        "ds_read_b64_tr_b16 a[54:55], v102 offset:22016\n\t"
        "v_xor_b32 v102, 49152, v101\n\t"
        "ds_read_b64_tr_b16 a[120:121], v102 offset:13312\n\t"
        "ds_read_b64_tr_b16 a[122:123], v102 offset:13824\n\t"
        "ds_read_b64_tr_b16 a[124:125], v102 offset:21504\n\t"
        "ds_read_b64_tr_b16 a[126:127], v102 offset:22016\n\t"
        "s_waitcnt vmcnt(4) lgkmcnt(0)\n\t"
        "s_barrier\n\t"
        "s_mov_b64 s[56:57], %[W1]\n\t"
        "s_mul_i32 s64, %[w], 524288\n\t"
        "s_add_u32 s64, s64, 0\n\t"
        "s_mul_i32 s65, %[w], 16384\n\t"
        "s_add_u32 s65, s65, 13312\n\t"
        "s_add_u32 m0, s65, 0\n\t"
        "s_nop 0\n\t"
        "buffer_load_dwordx4 v96, s[56:59], s64 offen lds\n\t"
        "s_add_u32 m0, s65, 1024\n\t"
        "s_add_u32 s64, s64, 32768\n\t"
        "buffer_load_dwordx4 v97, s[56:59], s64 offen lds\n\t"
        "s_add_u32 m0, s65, 2048\n\t"
        "s_add_u32 s64, s64, 32768\n\t"
        "buffer_load_dwordx4 v98, s[56:59], s64 offen lds\n\t"
        "s_add_u32 m0, s65, 3072\n\t"
        "s_add_u32 s64, s64, 32768\n\t"
        "buffer_load_dwordx4 v99, s[56:59], s64 offen lds\n\t"
        "s_add_u32 m0, s65, 4096\n\t"
        "s_add_u32 s64, s64, 32768\n\t"
        "buffer_load_dwordx4 v96, s[56:59], s64 offen lds\n\t"
        "s_add_u32 m0, s65, 5120\n\t"
        "s_add_u32 s64, s64, 32768\n\t"
        "buffer_load_dwordx4 v97, s[56:59], s64 offen lds\n\t"
        "s_add_u32 m0, s65, 6144\n\t"
        "s_add_u32 s64, s64, 32768\n\t"
        "buffer_load_dwordx4 v98, s[56:59], s64 offen lds\n\t"
        "s_add_u32 m0, s65, 7168\n\t"
        "s_add_u32 s64, s64, 32768\n\t"
        "buffer_load_dwordx4 v99, s[56:59], s64 offen lds\n\t"
        "s_add_u32 m0, s65, 8192\n\t"
        "s_add_u32 s64, s64, 32768\n\t"
        "buffer_load_dwordx4 v96, s[56:59], s64 offen lds\n\t"
        "s_add_u32 m0, s65, 9216\n\t"
        "s_add_u32 s64, s64, 32768\n\t"
        "buffer_load_dwordx4 v97, s[56:59], s64 offen lds\n\t"
        "s_add_u32 m0, s65, 10240\n\t"
        "s_add_u32 s64, s64, 32768\n\t"
        "buffer_load_dwordx4 v98, s[56:59], s64 offen lds\n\t"
        "s_add_u32 m0, s65, 11264\n\t"
        "s_add_u32 s64, s64, 32768\n\t"
        "buffer_load_dwordx4 v99, s[56:59], s64 offen lds\n\t"
        "s_add_u32 m0, s65, 12288\n\t"
        "s_add_u32 s64, s64, 32768\n\t"
        "buffer_load_dwordx4 v96, s[56:59], s64 offen lds\n\t"
        "s_add_u32 m0, s65, 13312\n\t"
        "s_add_u32 s64, s64, 32768\n\t"
        "buffer_load_dwordx4 v97, s[56:59], s64 offen lds\n\t"
        "s_add_u32 m0, s65, 14336\n\t"
        "s_add_u32 s64, s64, 32768\n\t"
        "buffer_load_dwordx4 v98, s[56:59], s64 offen lds\n\t"
        "s_add_u32 m0, s65, 15360\n\t"
        "s_add_u32 s64, s64, 32768\n\t"
        "buffer_load_dwordx4 v99, s[56:59], s64 offen lds\n\t"
        "v_xor_b32 v102, 0, v100\n\t"
        "v_add_u32 v102, 0x10000, v102\n\t"
        "ds_read_b64_tr_b16 a[8:9], v102 offset:13312\n\t"
        "ds_read_b64_tr_b16 a[10:11], v102 offset:13824\n\t"
        "ds_read_b64_tr_b16 a[12:13], v102 offset:21504\n\t"
        "ds_read_b64_tr_b16 a[14:15], v102 offset:22016\n\t"
        "v_xor_b32 v102, 0, v101\n\t"
        "v_add_u32 v102, 0x10000, v102\n\t"
        "ds_read_b64_tr_b16 a[80:81], v102 offset:13312\n\t"
        "ds_read_b64_tr_b16 a[82:83], v102 offset:13824\n\t"
        "ds_read_b64_tr_b16 a[84:85], v102 offset:21504\n\t"
        "ds_read_b64_tr_b16 a[86:87], v102 offset:22016\n\t"
        "v_xor_b32 v102, 16384, v100\n\t"
        "v_add_u32 v102, 0x10000, v102\n\t"
        "ds_read_b64_tr_b16 a[24:25], v102 offset:13312\n\t"
        "ds_read_b64_tr_b16 a[26:27], v102 offset:13824\n\t"
        "ds_read_b64_tr_b16 a[28:29], v102 offset:21504\n\t"
        "ds_read_b64_tr_b16 a[30:31], v102 offset:22016\n\t"
        "v_xor_b32 v102, 16384, v101\n\t"
        "v_add_u32 v102, 0x10000, v102\n\t"
        "ds_read_b64_tr_b16 a[96:97], v102 offset:13312\n\t"
        "ds_read_b64_tr_b16 a[98:99], v102 offset:13824\n\t"
        "ds_read_b64_tr_b16 a[100:101], v102 offset:21504\n\t"
        "ds_read_b64_tr_b16 a[102:103], v102 offset:22016\n\t"
        "v_xor_b32 v102, 32768, v100\n\t"
        "v_add_u32 v102, 0x10000, v102\n\t"
        "ds_read_b64_tr_b16 a[40:41], v102 offset:13312\n\t"
        "ds_read_b64_tr_b16 a[42:43], v102 offset:13824\n\t"
        "ds_read_b64_tr_b16 a[44:45], v102 offset:21504\n\t"
        "ds_read_b64_tr_b16 a[46:47], v102 offset:22016\n\t"
        "v_xor_b32 v102, 32768, v101\n\t"
        "v_add_u32 v102, 0x10000, v102\n\t"
        "ds_read_b64_tr_b16 a[112:113], v102 offset:13312\n\t"
        "ds_read_b64_tr_b16 a[114:115], v102 offset:13824\n\t"
        "ds_read_b64_tr_b16 a[116:117], v102 offset:21504\n\t"
        "ds_read_b64_tr_b16 a[118:119], v102 offset:22016\n\t"
        "v_xor_b32 v102, 49152, v100\n\t"
        "v_add_u32 v102, 0x10000, v102\n\t"
        "ds_read_b64_tr_b16 a[56:57], v102 offset:13312\n\t"
        "ds_read_b64_tr_b16 a[58:59], v102 offset:13824\n\t"
        "ds_read_b64_tr_b16 a[60:61], v102 offset:21504\n\t"
        "ds_read_b64_tr_b16 a[62:63], v102 offset:22016\n\t"
        "v_xor_b32 v102, 49152, v101\n\t"
        "v_add_u32 v102, 0x10000, v102\n\t"
        "ds_read_b64_tr_b16 a[128:129], v102 offset:13312\n\t"
        "ds_read_b64_tr_b16 a[130:131], v102 offset:13824\n\t"
        "ds_read_b64_tr_b16 a[132:133], v102 offset:21504\n\t"
        "ds_read_b64_tr_b16 a[134:135], v102 offset:22016\n\t"
        "s_waitcnt vmcnt(16) lgkmcnt(0)\n\t"
        "s_barrier\n\t"
        "s_mov_b64 s[56:57], %[W1]\n\t"
        "s_mul_i32 s64, %[w], 524288\n\t"
        "s_add_u32 s64, s64, 2097152\n\t"
        "s_mul_i32 s65, %[w], 16384\n\t"
        "s_add_u32 s65, s65, 78848\n\t"
        "s_add_u32 m0, s65, 0\n\t"
        "s_nop 0\n\t"
        "buffer_load_dwordx4 v96, s[56:59], s64 offen lds\n\t"
        "s_add_u32 m0, s65, 1024\n\t"
        "s_add_u32 s64, s64, 32768\n\t"
        "buffer_load_dwordx4 v97, s[56:59], s64 offen lds\n\t"
        "s_add_u32 m0, s65, 2048\n\t"
        "s_add_u32 s64, s64, 32768\n\t"
        "buffer_load_dwordx4 v98, s[56:59], s64 offen lds\n\t"
        "s_add_u32 m0, s65, 3072\n\t"
        "s_add_u32 s64, s64, 32768\n\t"
        "buffer_load_dwordx4 v99, s[56:59], s64 offen lds\n\t"
        "s_add_u32 m0, s65, 4096\n\t"
        "s_add_u32 s64, s64, 32768\n\t"
        "buffer_load_dwordx4 v96, s[56:59], s64 offen lds\n\t"
        "s_add_u32 m0, s65, 5120\n\t"
        "s_add_u32 s64, s64, 32768\n\t"
        "buffer_load_dwordx4 v97, s[56:59], s64 offen lds\n\t"
        "s_add_u32 m0, s65, 6144\n\t"
        "s_add_u32 s64, s64, 32768\n\t"
        "buffer_load_dwordx4 v98, s[56:59], s64 offen lds\n\t"
        "s_add_u32 m0, s65, 7168\n\t"
        "s_add_u32 s64, s64, 32768\n\t"
        "buffer_load_dwordx4 v99, s[56:59], s64 offen lds\n\t"
        "s_add_u32 m0, s65, 8192\n\t"
        "s_add_u32 s64, s64, 32768\n\t"
        "buffer_load_dwordx4 v96, s[56:59], s64 offen lds\n\t"
        "s_add_u32 m0, s65, 9216\n\t"
        "s_add_u32 s64, s64, 32768\n\t"
        "buffer_load_dwordx4 v97, s[56:59], s64 offen lds\n\t"
        "s_add_u32 m0, s65, 10240\n\t"
        "s_add_u32 s64, s64, 32768\n\t"
        "buffer_load_dwordx4 v98, s[56:59], s64 offen lds\n\t"
        "s_add_u32 m0, s65, 11264\n\t"
        "s_add_u32 s64, s64, 32768\n\t"
        "buffer_load_dwordx4 v99, s[56:59], s64 offen lds\n\t"
        "s_add_u32 m0, s65, 12288\n\t"
        "s_add_u32 s64, s64, 32768\n\t"
        "buffer_load_dwordx4 v96, s[56:59], s64 offen lds\n\t"
        "s_add_u32 m0, s65, 13312\n\t"
        "s_add_u32 s64, s64, 32768\n\t"
        "buffer_load_dwordx4 v97, s[56:59], s64 offen lds\n\t"
        "s_add_u32 m0, s65, 14336\n\t"
        "s_add_u32 s64, s64, 32768\n\t"
        "buffer_load_dwordx4 v98, s[56:59], s64 offen lds\n\t"
        "s_add_u32 m0, s65, 15360\n\t"
        "s_add_u32 s64, s64, 32768\n\t"
        "buffer_load_dwordx4 v99, s[56:59], s64 offen lds\n\t"
        "v_and_b32 v102, 0x3fff, v100\n\t"
        "v_add_u32 v102, 0x20000, v102\n\t"
        "ds_read_b64_tr_b16 a[64:65], v102 offset:13312\n\t"
        "ds_read_b64_tr_b16 a[66:67], v102 offset:13824\n\t"
        "ds_read_b64_tr_b16 a[68:69], v102 offset:21504\n\t"
        "ds_read_b64_tr_b16 a[70:71], v102 offset:22016\n\t"
        "v_and_b32 v102, 0x3fff, v101\n\t"
        "v_add_u32 v102, 0x20000, v102\n\t"
        "ds_read_b64_tr_b16 a[136:137], v102 offset:13312\n\t"
        "ds_read_b64_tr_b16 a[138:139], v102 offset:13824\n\t"
        "ds_read_b64_tr_b16 a[140:141], v102 offset:21504\n\t"
        "ds_read_b64_tr_b16 a[142:143], v102 offset:22016\n\t"
        "s_waitcnt vmcnt(16) lgkmcnt(0)\n\t"
        "s_barrier\n\t"
        "s_mov_b64 s[56:57], %[W1]\n\t"
        "s_mul_i32 s64, %[w], 131072\n\t"
        "s_add_u32 s64, s64, 4194304\n\t"
        "s_mul_i32 s65, %[w], 4096\n\t"
        "s_add_u32 s65, s65, 144384\n\t"
        "s_add_u32 m0, s65, 0\n\t"
        "s_nop 0\n\t"
        "buffer_load_dwordx4 v96, s[56:59], s64 offen lds\n\t"
        "s_add_u32 m0, s65, 1024\n\t"
        "s_add_u32 s64, s64, 32768\n\t"
        "buffer_load_dwordx4 v97, s[56:59], s64 offen lds\n\t"
        "s_add_u32 m0, s65, 2048\n\t"
        "s_add_u32 s64, s64, 32768\n\t"
        "buffer_load_dwordx4 v98, s[56:59], s64 offen lds\n\t"
        "s_add_u32 m0, s65, 3072\n\t"
        "s_add_u32 s64, s64, 32768\n\t"
        "buffer_load_dwordx4 v99, s[56:59], s64 offen lds\n\t"
        "v_xor_b32 v102, 0, v100\n\t"
        "ds_read_b64_tr_b16 a[192:193], v102 offset:13312\n\t"
        "ds_read_b64_tr_b16 a[194:195], v102 offset:13824\n\t"
        "ds_read_b64_tr_b16 a[196:197], v102 offset:21504\n\t"
        "ds_read_b64_tr_b16 a[198:199], v102 offset:22016\n\t"
        "v_xor_b32 v102, 0, v101\n\t"
        "ds_read_b64_tr_b16 v[24:25], v102 offset:13312\n\t"
        "ds_read_b64_tr_b16 v[26:27], v102 offset:13824\n\t"
        "ds_read_b64_tr_b16 v[28:29], v102 offset:21504\n\t"
        "ds_read_b64_tr_b16 v[30:31], v102 offset:22016\n\t"
        "v_xor_b32 v102, 16384, v100\n\t"
        "ds_read_b64_tr_b16 a[208:209], v102 offset:13312\n\t"
        "ds_read_b64_tr_b16 a[210:211], v102 offset:13824\n\t"
        "ds_read_b64_tr_b16 a[212:213], v102 offset:21504\n\t"
        "ds_read_b64_tr_b16 a[214:215], v102 offset:22016\n\t"
        "v_xor_b32 v102, 16384, v101\n\t"
        "ds_read_b64_tr_b16 v[40:41], v102 offset:13312\n\t"
        "ds_read_b64_tr_b16 v[42:43], v102 offset:13824\n\t"
        "ds_read_b64_tr_b16 v[44:45], v102 offset:21504\n\t"
        "ds_read_b64_tr_b16 v[46:47], v102 offset:22016\n\t"
        "v_xor_b32 v102, 32768, v100\n\t"
        "ds_read_b64_tr_b16 a[224:225], v102 offset:13312\n\t"
        "ds_read_b64_tr_b16 a[226:227], v102 offset:13824\n\t"
        "ds_read_b64_tr_b16 a[228:229], v102 offset:21504\n\t"
        "ds_read_b64_tr_b16 a[230:231], v102 offset:22016\n\t"
        "v_xor_b32 v102, 32768, v101\n\t"
        "ds_read_b64_tr_b16 v[56:57], v102 offset:13312\n\t"
        "ds_read_b64_tr_b16 v[58:59], v102 offset:13824\n\t"
        "ds_read_b64_tr_b16 v[60:61], v102 offset:21504\n\t"
        "ds_read_b64_tr_b16 v[62:63], v102 offset:22016\n\t"
        "v_xor_b32 v102, 49152, v100\n\t"
        "ds_read_b64_tr_b16 a[240:241], v102 offset:13312\n\t"
        "ds_read_b64_tr_b16 a[242:243], v102 offset:13824\n\t"
        "ds_read_b64_tr_b16 a[244:245], v102 offset:21504\n\t"
        "ds_read_b64_tr_b16 a[246:247], v102 offset:22016\n\t"
        "v_xor_b32 v102, 49152, v101\n\t"
        "ds_read_b64_tr_b16 v[72:73], v102 offset:13312\n\t"
        "ds_read_b64_tr_b16 v[74:75], v102 offset:13824\n\t"
        "ds_read_b64_tr_b16 v[76:77], v102 offset:21504\n\t"
        "ds_read_b64_tr_b16 v[78:79], v102 offset:22016\n\t"
        "s_waitcnt vmcnt(4) lgkmcnt(0)\n\t"
        "s_barrier\n\t"
        "s_mov_b64 s[56:57], %[W2]\n\t"
        "s_mul_i32 s64, %[w], 524288\n\t"
        "s_add_u32 s64, s64, 0\n\t"
        "s_mul_i32 s65, %[w], 16384\n\t"
        "s_add_u32 s65, s65, 13312\n\t"
        "s_add_u32 m0, s65, 0\n\t"
        "s_nop 0\n\t"
        "buffer_load_dwordx4 v96, s[56:59], s64 offen lds\n\t"
        "s_add_u32 m0, s65, 1024\n\t"
        "s_add_u32 s64, s64, 32768\n\t"
        "buffer_load_dwordx4 v97, s[56:59], s64 offen lds\n\t"
        "s_add_u32 m0, s65, 2048\n\t"
        "s_add_u32 s64, s64, 32768\n\t"
        "buffer_load_dwordx4 v98, s[56:59], s64 offen lds\n\t"
        "s_add_u32 m0, s65, 3072\n\t"
        "s_add_u32 s64, s64, 32768\n\t"
        "buffer_load_dwordx4 v99, s[56:59], s64 offen lds\n\t"
        "s_add_u32 m0, s65, 4096\n\t"
        "s_add_u32 s64, s64, 32768\n\t"
        "buffer_load_dwordx4 v96, s[56:59], s64 offen lds\n\t"
        "s_add_u32 m0, s65, 5120\n\t"
        "s_add_u32 s64, s64, 32768\n\t"
        "buffer_load_dwordx4 v97, s[56:59], s64 offen lds\n\t"
        "s_add_u32 m0, s65, 6144\n\t"
        "s_add_u32 s64, s64, 32768\n\t"
        "buffer_load_dwordx4 v98, s[56:59], s64 offen lds\n\t"
        "s_add_u32 m0, s65, 7168\n\t"
        "s_add_u32 s64, s64, 32768\n\t"
        "buffer_load_dwordx4 v99, s[56:59], s64 offen lds\n\t"
        "s_add_u32 m0, s65, 8192\n\t"
        "s_add_u32 s64, s64, 32768\n\t"
        "buffer_load_dwordx4 v96, s[56:59], s64 offen lds\n\t"
        "s_add_u32 m0, s65, 9216\n\t"
        "s_add_u32 s64, s64, 32768\n\t"
        "buffer_load_dwordx4 v97, s[56:59], s64 offen lds\n\t"
        "s_add_u32 m0, s65, 10240\n\t"
        "s_add_u32 s64, s64, 32768\n\t"
        "buffer_load_dwordx4 v98, s[56:59], s64 offen lds\n\t"
        "s_add_u32 m0, s65, 11264\n\t"
        "s_add_u32 s64, s64, 32768\n\t"
        "buffer_load_dwordx4 v99, s[56:59], s64 offen lds\n\t"
        "s_add_u32 m0, s65, 12288\n\t"
        "s_add_u32 s64, s64, 32768\n\t"
        "buffer_load_dwordx4 v96, s[56:59], s64 offen lds\n\t"
        "s_add_u32 m0, s65, 13312\n\t"
        "s_add_u32 s64, s64, 32768\n\t"
        "buffer_load_dwordx4 v97, s[56:59], s64 offen lds\n\t"
        "s_add_u32 m0, s65, 14336\n\t"
        "s_add_u32 s64, s64, 32768\n\t"
        "buffer_load_dwordx4 v98, s[56:59], s64 offen lds\n\t"
        "s_add_u32 m0, s65, 15360\n\t"
        "s_add_u32 s64, s64, 32768\n\t"
        "buffer_load_dwordx4 v99, s[56:59], s64 offen lds\n\t"
        "v_xor_b32 v102, 0, v100\n\t"
        "v_add_u32 v102, 0x10000, v102\n\t"
        "ds_read_b64_tr_b16 a[200:201], v102 offset:13312\n\t"
        "ds_read_b64_tr_b16 a[202:203], v102 offset:13824\n\t"
        "ds_read_b64_tr_b16 a[204:205], v102 offset:21504\n\t"
        "ds_read_b64_tr_b16 a[206:207], v102 offset:22016\n\t"
        "v_xor_b32 v102, 0, v101\n\t"
        "v_add_u32 v102, 0x10000, v102\n\t"
        "ds_read_b64_tr_b16 v[32:33], v102 offset:13312\n\t"
        "ds_read_b64_tr_b16 v[34:35], v102 offset:13824\n\t"
        "ds_read_b64_tr_b16 v[36:37], v102 offset:21504\n\t"
        "ds_read_b64_tr_b16 v[38:39], v102 offset:22016\n\t"
        "v_xor_b32 v102, 16384, v100\n\t"
        "v_add_u32 v102, 0x10000, v102\n\t"
        "ds_read_b64_tr_b16 a[216:217], v102 offset:13312\n\t"
        "ds_read_b64_tr_b16 a[218:219], v102 offset:13824\n\t"
        "ds_read_b64_tr_b16 a[220:221], v102 offset:21504\n\t"
        "ds_read_b64_tr_b16 a[222:223], v102 offset:22016\n\t"
        "v_xor_b32 v102, 16384, v101\n\t"
        "v_add_u32 v102, 0x10000, v102\n\t"
        "ds_read_b64_tr_b16 v[48:49], v102 offset:13312\n\t"
        "ds_read_b64_tr_b16 v[50:51], v102 offset:13824\n\t"
        "ds_read_b64_tr_b16 v[52:53], v102 offset:21504\n\t"
        "ds_read_b64_tr_b16 v[54:55], v102 offset:22016\n\t"
        "v_xor_b32 v102, 32768, v100\n\t"
        "v_add_u32 v102, 0x10000, v102\n\t"
        "ds_read_b64_tr_b16 a[232:233], v102 offset:13312\n\t"
        "ds_read_b64_tr_b16 a[234:235], v102 offset:13824\n\t"
        "ds_read_b64_tr_b16 a[236:237], v102 offset:21504\n\t"
        "ds_read_b64_tr_b16 a[238:239], v102 offset:22016\n\t"
        "v_xor_b32 v102, 32768, v101\n\t"
        "v_add_u32 v102, 0x10000, v102\n\t"
        "ds_read_b64_tr_b16 v[64:65], v102 offset:13312\n\t"
        "ds_read_b64_tr_b16 v[66:67], v102 offset:13824\n\t"
        "ds_read_b64_tr_b16 v[68:69], v102 offset:21504\n\t"
        "ds_read_b64_tr_b16 v[70:71], v102 offset:22016\n\t"
        "v_xor_b32 v102, 49152, v100\n\t"
        "v_add_u32 v102, 0x10000, v102\n\t"
        "ds_read_b64_tr_b16 a[248:249], v102 offset:13312\n\t"
        "ds_read_b64_tr_b16 a[250:251], v102 offset:13824\n\t"
        "ds_read_b64_tr_b16 a[252:253], v102 offset:21504\n\t"
        "ds_read_b64_tr_b16 a[254:255], v102 offset:22016\n\t"
        "v_xor_b32 v102, 49152, v101\n\t"
        "v_add_u32 v102, 0x10000, v102\n\t"
        "ds_read_b64_tr_b16 v[80:81], v102 offset:13312\n\t"
        "ds_read_b64_tr_b16 v[82:83], v102 offset:13824\n\t"
        "ds_read_b64_tr_b16 v[84:85], v102 offset:21504\n\t"
        "ds_read_b64_tr_b16 v[86:87], v102 offset:22016\n\t"
        "s_waitcnt vmcnt(16) lgkmcnt(0)\n\t"
        "s_barrier\n\t"
        "s_mov_b64 s[56:57], %[W2]\n\t"
        "s_mul_i32 s64, %[w], 524288\n\t"
        "s_add_u32 s64, s64, 2097152\n\t"
        "s_mul_i32 s65, %[w], 16384\n\t"
        "s_add_u32 s65, s65, 78848\n\t"
        "s_add_u32 m0, s65, 0\n\t"
        "s_nop 0\n\t"
        "buffer_load_dwordx4 v96, s[56:59], s64 offen lds\n\t"
        "s_add_u32 m0, s65, 1024\n\t"
        "s_add_u32 s64, s64, 32768\n\t"
        "buffer_load_dwordx4 v97, s[56:59], s64 offen lds\n\t"
        "s_add_u32 m0, s65, 2048\n\t"
        "s_add_u32 s64, s64, 32768\n\t"
        "buffer_load_dwordx4 v98, s[56:59], s64 offen lds\n\t"
        "s_add_u32 m0, s65, 3072\n\t"
        "s_add_u32 s64, s64, 32768\n\t"
        "buffer_load_dwordx4 v99, s[56:59], s64 offen lds\n\t"
        "s_add_u32 m0, s65, 4096\n\t"
        "s_add_u32 s64, s64, 32768\n\t"
        "buffer_load_dwordx4 v96, s[56:59], s64 offen lds\n\t"
        "s_add_u32 m0, s65, 5120\n\t"
        "s_add_u32 s64, s64, 32768\n\t"
        "buffer_load_dwordx4 v97, s[56:59], s64 offen lds\n\t"
        "s_add_u32 m0, s65, 6144\n\t"
        "s_add_u32 s64, s64, 32768\n\t"
        "buffer_load_dwordx4 v98, s[56:59], s64 offen lds\n\t"
        "s_add_u32 m0, s65, 7168\n\t"
        "s_add_u32 s64, s64, 32768\n\t"
        "buffer_load_dwordx4 v99, s[56:59], s64 offen lds\n\t"
        "s_add_u32 m0, s65, 8192\n\t"
        "s_add_u32 s64, s64, 32768\n\t"
        "buffer_load_dwordx4 v96, s[56:59], s64 offen lds\n\t"
        "s_add_u32 m0, s65, 9216\n\t"
        "s_add_u32 s64, s64, 32768\n\t"
        "buffer_load_dwordx4 v97, s[56:59], s64 offen lds\n\t"
        "s_add_u32 m0, s65, 10240\n\t"
        "s_add_u32 s64, s64, 32768\n\t"
        "buffer_load_dwordx4 v98, s[56:59], s64 offen lds\n\t"
        "s_add_u32 m0, s65, 11264\n\t"
        "s_add_u32 s64, s64, 32768\n\t"
        "buffer_load_dwordx4 v99, s[56:59], s64 offen lds\n\t"
        "s_add_u32 m0, s65, 12288\n\t"
        "s_add_u32 s64, s64, 32768\n\t"
        "buffer_load_dwordx4 v96, s[56:59], s64 offen lds\n\t"
        "s_add_u32 m0, s65, 13312\n\t"
        "s_add_u32 s64, s64, 32768\n\t"
        "buffer_load_dwordx4 v97, s[56:59], s64 offen lds\n\t"
        "s_add_u32 m0, s65, 14336\n\t"
        "s_add_u32 s64, s64, 32768\n\t"
        "buffer_load_dwordx4 v98, s[56:59], s64 offen lds\n\t"
        "s_add_u32 m0, s65, 15360\n\t"
        "s_add_u32 s64, s64, 32768\n\t"
        "buffer_load_dwordx4 v99, s[56:59], s64 offen lds\n\t"
        "v_and_b32 v102, 0x3fff, v100\n\t"
        "v_add_u32 v102, 0x20000, v102\n\t"
        "ds_read_b64_tr_b16 v[16:17], v102 offset:13312\n\t"
        "ds_read_b64_tr_b16 v[18:19], v102 offset:13824\n\t"
        "ds_read_b64_tr_b16 v[20:21], v102 offset:21504\n\t"
        "ds_read_b64_tr_b16 v[22:23], v102 offset:22016\n\t"
        "v_and_b32 v102, 0x3fff, v101\n\t"
        "v_add_u32 v102, 0x20000, v102\n\t"
        "ds_read_b64_tr_b16 v[88:89], v102 offset:13312\n\t"
        "ds_read_b64_tr_b16 v[90:91], v102 offset:13824\n\t"
        "ds_read_b64_tr_b16 v[92:93], v102 offset:21504\n\t"
        "ds_read_b64_tr_b16 v[94:95], v102 offset:22016\n\t"
        "s_waitcnt vmcnt(16) lgkmcnt(0)\n\t"
        "s_barrier\n\t"
        "s_mov_b64 s[56:57], %[W2]\n\t"
        "s_mul_i32 s64, %[w], 131072\n\t"
        "s_add_u32 s64, s64, 4194304\n\t"
        "s_mul_i32 s65, %[w], 4096\n\t"
        "s_add_u32 s65, s65, 144384\n\t"
        "s_add_u32 m0, s65, 0\n\t"
        "s_nop 0\n\t"
        "buffer_load_dwordx4 v96, s[56:59], s64 offen lds\n\t"
        "s_add_u32 m0, s65, 1024\n\t"
        "s_add_u32 s64, s64, 32768\n\t"
        "buffer_load_dwordx4 v97, s[56:59], s64 offen lds\n\t"
        "s_add_u32 m0, s65, 2048\n\t"
        "s_add_u32 s64, s64, 32768\n\t"
        "buffer_load_dwordx4 v98, s[56:59], s64 offen lds\n\t"
        "s_add_u32 m0, s65, 3072\n\t"
        "s_add_u32 s64, s64, 32768\n\t"
        "buffer_load_dwordx4 v99, s[56:59], s64 offen lds\n\t"
        "v_xor_b32 v102, 0, v100\n\t"
        "ds_read_b64_tr_b16 a[144:145], v102 offset:13312\n\t"
        "ds_read_b64_tr_b16 a[146:147], v102 offset:13824\n\t"
        "ds_read_b64_tr_b16 a[148:149], v102 offset:21504\n\t"
        "ds_read_b64_tr_b16 a[150:151], v102 offset:22016\n\t"
        "v_xor_b32 v102, 0, v101\n\t"
        "ds_read_b64_tr_b16 v[112:113], v102 offset:13312\n\t"
        "ds_read_b64_tr_b16 v[114:115], v102 offset:13824\n\t"
        "ds_read_b64_tr_b16 v[116:117], v102 offset:21504\n\t"
        "ds_read_b64_tr_b16 v[118:119], v102 offset:22016\n\t"
        "v_xor_b32 v102, 16384, v100\n\t"
        "ds_read_b64_tr_b16 a[160:161], v102 offset:13312\n\t"
        "ds_read_b64_tr_b16 a[162:163], v102 offset:13824\n\t"
        "ds_read_b64_tr_b16 a[164:165], v102 offset:21504\n\t"
        "ds_read_b64_tr_b16 a[166:167], v102 offset:22016\n\t"
        "v_xor_b32 v102, 16384, v101\n\t"
        "ds_read_b64_tr_b16 v[128:129], v102 offset:13312\n\t"
        "ds_read_b64_tr_b16 v[130:131], v102 offset:13824\n\t"
        "ds_read_b64_tr_b16 v[132:133], v102 offset:21504\n\t"
        "ds_read_b64_tr_b16 v[134:135], v102 offset:22016\n\t"
        "v_xor_b32 v102, 32768, v100\n\t"
        "ds_read_b64_tr_b16 a[176:177], v102 offset:13312\n\t"
        "ds_read_b64_tr_b16 a[178:179], v102 offset:13824\n\t"
        "ds_read_b64_tr_b16 a[180:181], v102 offset:21504\n\t"
        "ds_read_b64_tr_b16 a[182:183], v102 offset:22016\n\t"
        "v_xor_b32 v102, 32768, v101\n\t"
        "ds_read_b64_tr_b16 v[144:145], v102 offset:13312\n\t"
        "ds_read_b64_tr_b16 v[146:147], v102 offset:13824\n\t"
        "ds_read_b64_tr_b16 v[148:149], v102 offset:21504\n\t"
        "ds_read_b64_tr_b16 v[150:151], v102 offset:22016\n\t"
        "v_xor_b32 v102, 49152, v100\n\t"
        "ds_read_b64_tr_b16 v[160:161], v102 offset:13312\n\t"
        "ds_read_b64_tr_b16 v[162:163], v102 offset:13824\n\t"
        "ds_read_b64_tr_b16 v[164:165], v102 offset:21504\n\t"
        "ds_read_b64_tr_b16 v[166:167], v102 offset:22016\n\t"
        "v_xor_b32 v102, 49152, v101\n\t"
        "ds_read_b64_tr_b16 v[168:169], v102 offset:13312\n\t"
        "ds_read_b64_tr_b16 v[170:171], v102 offset:13824\n\t"
        "ds_read_b64_tr_b16 v[172:173], v102 offset:21504\n\t"
        "ds_read_b64_tr_b16 v[174:175], v102 offset:22016\n\t"
        "s_waitcnt vmcnt(4) lgkmcnt(0)\n\t"
        "s_barrier\n\t"
        "v_xor_b32 v102, 0, v100\n\t"
        "v_add_u32 v102, 0x10000, v102\n\t"
        "ds_read_b64_tr_b16 a[152:153], v102 offset:13312\n\t"
        "ds_read_b64_tr_b16 a[154:155], v102 offset:13824\n\t"
        "ds_read_b64_tr_b16 a[156:157], v102 offset:21504\n\t"
        "ds_read_b64_tr_b16 a[158:159], v102 offset:22016\n\t"
        "v_xor_b32 v102, 0, v101\n\t"
        "v_add_u32 v102, 0x10000, v102\n\t"
        "ds_read_b64_tr_b16 v[120:121], v102 offset:13312\n\t"
        "ds_read_b64_tr_b16 v[122:123], v102 offset:13824\n\t"
        "ds_read_b64_tr_b16 v[124:125], v102 offset:21504\n\t"
        "ds_read_b64_tr_b16 v[126:127], v102 offset:22016\n\t"
        "v_xor_b32 v102, 16384, v100\n\t"
        "v_add_u32 v102, 0x10000, v102\n\t"
        "ds_read_b64_tr_b16 a[168:169], v102 offset:13312\n\t"
        "ds_read_b64_tr_b16 a[170:171], v102 offset:13824\n\t"
        "ds_read_b64_tr_b16 a[172:173], v102 offset:21504\n\t"
        "ds_read_b64_tr_b16 a[174:175], v102 offset:22016\n\t"
        "v_xor_b32 v102, 16384, v101\n\t"
        "v_add_u32 v102, 0x10000, v102\n\t"
        "ds_read_b64_tr_b16 v[136:137], v102 offset:13312\n\t"
        "ds_read_b64_tr_b16 v[138:139], v102 offset:13824\n\t"
        "ds_read_b64_tr_b16 v[140:141], v102 offset:21504\n\t"
        "ds_read_b64_tr_b16 v[142:143], v102 offset:22016\n\t"
        "v_xor_b32 v102, 32768, v100\n\t"
        "v_add_u32 v102, 0x10000, v102\n\t"
        "ds_read_b64_tr_b16 a[184:185], v102 offset:13312\n\t"
        "ds_read_b64_tr_b16 a[186:187], v102 offset:13824\n\t"
        "ds_read_b64_tr_b16 a[188:189], v102 offset:21504\n\t"
        "ds_read_b64_tr_b16 a[190:191], v102 offset:22016\n\t"
        "v_xor_b32 v102, 32768, v101\n\t"
        "v_add_u32 v102, 0x10000, v102\n\t"
        "ds_read_b64_tr_b16 v[152:153], v102 offset:13312\n\t"
        "ds_read_b64_tr_b16 v[154:155], v102 offset:13824\n\t"
        "ds_read_b64_tr_b16 v[156:157], v102 offset:21504\n\t"
        "ds_read_b64_tr_b16 v[158:159], v102 offset:22016\n\t"
        "v_xor_b32 v102, 49152, v100\n\t"
        "v_add_u32 v102, 0x10000, v102\n\t"
        "ds_read_b64_tr_b16 v[176:177], v102 offset:13312\n\t"
        "ds_read_b64_tr_b16 v[178:179], v102 offset:13824\n\t"
        "ds_read_b64_tr_b16 v[180:181], v102 offset:21504\n\t"
        "ds_read_b64_tr_b16 v[182:183], v102 offset:22016\n\t"
        "v_xor_b32 v102, 49152, v101\n\t"
        "v_add_u32 v102, 0x10000, v102\n\t"
        "ds_read_b64_tr_b16 v[184:185], v102 offset:13312\n\t"
        "ds_read_b64_tr_b16 v[186:187], v102 offset:13824\n\t"
        "ds_read_b64_tr_b16 v[188:189], v102 offset:21504\n\t"
        "ds_read_b64_tr_b16 v[190:191], v102 offset:22016\n\t"
        "s_waitcnt vmcnt(0) lgkmcnt(0)\n\t"
        "s_barrier\n\t"
        "v_and_b32 v102, 0x3fff, v100\n\t"
        "v_add_u32 v102, 0x20000, v102\n\t"
        "ds_read_b64_tr_b16 v[192:193], v102 offset:13312\n\t"
        "ds_read_b64_tr_b16 v[194:195], v102 offset:13824\n\t"
        "ds_read_b64_tr_b16 v[196:197], v102 offset:21504\n\t"
        "ds_read_b64_tr_b16 v[198:199], v102 offset:22016\n\t"
        "v_and_b32 v102, 0x3fff, v101\n\t"
        "v_add_u32 v102, 0x20000, v102\n\t"
        "ds_read_b64_tr_b16 v[200:201], v102 offset:13312\n\t"
        "ds_read_b64_tr_b16 v[202:203], v102 offset:13824\n\t"
        "ds_read_b64_tr_b16 v[204:205], v102 offset:21504\n\t"
        "ds_read_b64_tr_b16 v[206:207], v102 offset:22016\n\t"
        "s_waitcnt lgkmcnt(0)\n\t"
        "s_barrier\n\t"
        "ds_write_b128 %[wl], v[112:115] offset:0\n\t"
        "ds_write_b128 %[wl], v[116:119] offset:1024\n\t"
        "ds_write_b128 %[wl], v[120:123] offset:2048\n\t"
        "ds_write_b128 %[wl], v[124:127] offset:3072\n\t"
        "ds_write_b128 %[wl], v[128:131] offset:4096\n\t"
        "ds_write_b128 %[wl], v[132:135] offset:5120\n\t"
        "ds_write_b128 %[wl], v[136:139] offset:6144\n\t"
        "ds_write_b128 %[wl], v[140:143] offset:7168\n\t"
        "ds_write_b128 %[wl], v[144:147] offset:8192\n\t"
        "ds_write_b128 %[wl], v[148:151] offset:9216\n\t"
        "ds_write_b128 %[wl], v[152:155] offset:10240\n\t"
        "ds_write_b128 %[wl], v[156:159] offset:11264\n\t"
        "ds_write_b128 %[wl], v[160:163] offset:12288\n\t"
        "ds_write_b128 %[wl], v[164:167] offset:13312\n\t"
        "ds_write_b128 %[wl], v[168:171] offset:14336\n\t"
        "ds_write_b128 %[wl], v[172:175] offset:15360\n\t"
        "ds_write_b128 %[wl], v[176:179] offset:16384\n\t"
        "ds_write_b128 %[wl], v[180:183] offset:17408\n\t"
        "ds_write_b128 %[wl], v[184:187] offset:18432\n\t"
        "ds_write_b128 %[wl], v[188:191] offset:19456\n\t"
        "ds_write_b128 %[wl], v[192:195] offset:20480\n\t"
        "ds_write_b128 %[wl], v[196:199] offset:21504\n\t"
        "ds_write_b128 %[wl], v[200:203] offset:22528\n\t"
        "ds_write_b128 %[wl], v[204:207] offset:23552\n\t"
        "s_mov_b32 s64, 0\n\t"
        "v_lshrrev_b32 v97, 2, %[l16]\n\t"
        "v_min_u32 v98, 28, v97\n\t"
        "v_add_u32 v98, 256, v98\n\t"
        "HPH%=:\n\t"
        "buffer_load_dword v96, off, %[RW], 0 sc1\n\t"
        "buffer_load_dword v99, v97, %[RW], %[ords] offen sc1\n\t"
        "buffer_load_dword v100, v98, %[RW], %[ords] offen sc1\n\t"
        "s_waitcnt vmcnt(0)\n\t"
        "v_readfirstlane_b32 s65, v96\n\t"
        "v_cmp_ne_u32 vcc, %[ep], v99\n\t"
        "v_cmp_ne_u32 s[66:67], %[ep], v100\n\t"
        "s_add_u32 s64, s64, 1\n\t"
        "s_cmp_ge_u32 s64, 0x100000\n\t"
        "s_cbranch_scc1 HPD%=\n\t"
        "s_or_b64 vcc, vcc, s[66:67]\n\t"
        "s_cbranch_scc1 HPH%=\n\t"
        "s_sub_u32 s65, s65, %[ep]\n\t"
        "s_cmp_gt_u32 s65, 1\n\t"
        "s_cbranch_scc1 HPH%=\n\t"
        "HPD%=:\n\t"
        "s_and_b32 s65, s65, 1\n\t"
        "s_lshl_b32 s65, s65, 13\n\t"
        "s_mov_b64 s[60:61], %[comm]\n\t"
        "s_add_u32 s60, s60, s65\n\t"
        "s_addc_u32 s61, s61, 0\n\t"
        "s_mov_b32 s62, 0x7fffffff\n\t"
        "s_mov_b32 s63, 0x00020000\n\t"
        "v_mov_b32 v96, -1\n\t"
        "v_mov_b32 v97, -1\n\t"
        "v_mov_b32 v98, -1\n\t"
        "v_mov_b32 v99, -1\n\t"
        "buffer_store_dwordx4 v[96:99], %[ooff], s[60:63], 0 offen sc1\n\t"
        "s_mov_b32 s64, 0\n\t"
        "s_mov_b32 s65, 0\n\t"
        "s_mov_b32 s77, 0\n\t"
        "s_mov_b32 s94, -1\n\t"
        "s_mov_b32 s95, 0x3e0293ee\n\t"
        "s_mov_b32 s97, 52\n\t"
        "s_mov_b32 s59, 0\n\t"
        "s_mov_b32 s66, %[kx0]\n\t"
        "s_mov_b32 s68, %[vx0]\n\t"
        "s_mov_b32 s69, 0x80000000\n\t"
        "s_mov_b32 s70, 0x80000000\n\t"
        "s_memrealtime s[82:83]\n\t"
        "s_memtime s[50:51]\n\t"
        "s_waitcnt lgkmcnt(0)\n\t"
        "s_mov_b32 s52, s82\n\t"
        "s_mov_b32 s53, s50\n\t"
        "s_mov_b32 s54, 6400\n\t"
        "s_mov_b32 s55, 9060\n\t"
        "HLOOP%=:\n\t"
        "buffer_load_dwordx2 v[108:109], %[fboff], s[60:63], 0 offen sc1\n\t"
        "s_lshl_b32 s79, s64, 7\n\t"
        "s_add_u32 s79, s79, %[kvfs]\n\t"
        "s_mov_b32 s76, 0\n\t"
        "s_cmp_eq_u32 s94, s64\n\t"
        "s_cbranch_scc1 HKVD%=\n\t"
        "HKV%=:\n\t"
        "buffer_load_dword v244, off, %[RW], s79 sc1\n\t"
        "s_waitcnt vmcnt(0)\n\t"
        "v_cmp_eq_u32 vcc, %[ep], v244\n\t"
        "s_cbranch_vccnz HKVD%=\n\t"
        "s_add_u32 s76, s76, 1\n\t"
        "s_or_b32 s71, s76, s77\n\t"
        "s_cmp_lt_u32 s71, 0x40000\n\t"
        "s_cbranch_scc1 HKV%=\n\t"
        "s_mov_b32 s77, 0x40000\n\t"
        "HKVD%=:\n\t"
        "buffer_load_dword v250, off, %[RW], s79 offset:128 sc1\n\t"
        "buffer_load_dwordx4 v[132:135], %[l16], %[RW], s66 offen sc1\n\t"
        "buffer_load_dwordx4 v[136:139], %[l16], %[RW], s66 offen offset:1024 sc1\n\t"
        "buffer_load_dwordx4 v[164:167], %[l16], %[RW], s68 offen sc1\n\t"
        "buffer_load_dwordx4 v[168:171], %[l16], %[RW], s68 offen offset:1024 sc1\n\t"
        "s_lshl_b32 s78, %[w], 9\n\t"
        "v_add_u32 v244, s78, %[krd]\n\t"
        "ds_read_b128 v[196:199], v244\n\t"
        "v_permlane16_swap_b32 v188, v192\n\t"
        "v_permlane16_swap_b32 v189, v193\n\t"
        "v_permlane16_swap_b32 v190, v194\n\t"
        "v_permlane16_swap_b32 v191, v195\n\t"
        "v_add_f32 v188, v188, v192\n\t"
        "v_add_f32 v189, v189, v193\n\t"
        "v_add_f32 v190, v190, v194\n\t"
        "v_add_f32 v191, v191, v195\n\t"
        "v_cvt_pk_bf16_f32 v220, v188, v189\n\t"
        "v_cvt_pk_bf16_f32 v221, v190, v191\n\t"
        "buffer_store_dwordx2 v[220:221], %[vmoff], %[RW], s70 offen sc1\n\t"
        "s_waitcnt lgkmcnt(0)\n\t"
        "buffer_store_dwordx4 v[196:199], %[kmoff], %[RW], s69 offen sc1\n\t"
        "s_lshl_b32 s78, s64, 16\n\t"
        "s_add_u32 s69, %[km0], s78\n\t"
        "s_add_u32 s70, %[vm0], s78\n\t"
        "v_mov_b32 v244, -1\n\t"
        "v_mov_b32 v245, -1\n\t"
        "v_mov_b32 v246, -1\n\t"
        "v_mov_b32 v247, -1\n\t"
        "s_add_u32 s78, s65, 1\n\t"
        "s_cmp_eq_u32 s78, 3\n\t"
        "s_cselect_b32 s78, 0, s78\n\t"
        "s_lshl_b32 s75, s78, 17\n\t"
        "s_lshl_b32 s74, s65, 17\n\t"
        "buffer_store_dwordx4 v[244:247], %[ooff], s[60:63], s75 offen sc1\n\t"
        "v_mov_b32 v172, 0\n\t"
        "v_mov_b32 v173, 0\n\t"
        "v_mov_b32 v174, 0\n\t"
        "v_mov_b32 v175, 0\n\t"
        "v_mov_b32 v176, 0\n\t"
        "v_mov_b32 v177, 0\n\t"
        "v_mov_b32 v178, 0\n\t"
        "v_mov_b32 v179, 0\n\t"
        "v_mov_b32 v180, 0\n\t"
        "v_mov_b32 v181, 0\n\t"
        "v_mov_b32 v182, 0\n\t"
        "v_mov_b32 v183, 0\n\t"
        "v_mov_b32 v184, 0\n\t"
        "v_mov_b32 v185, 0\n\t"
        "v_mov_b32 v186, 0\n\t"
        "v_mov_b32 v187, 0\n\t"
        "v_mov_b32 v188, 0\n\t"
        "v_mov_b32 v189, 0\n\t"
        "v_mov_b32 v190, 0\n\t"
        "v_mov_b32 v191, 0\n\t"
        "v_mov_b32 v192, 0\n\t"
        "v_mov_b32 v193, 0\n\t"
        "v_mov_b32 v194, 0\n\t"
        "v_mov_b32 v195, 0\n\t"
        "ds_read_b128 v[196:199], %[wl] offset:0\n\t"
        "ds_read_b128 v[200:203], %[wl] offset:1024\n\t"
        "ds_read_b128 v[204:207], %[wl] offset:2048\n\t"
        "ds_read_b128 v[208:211], %[wl] offset:3072\n\t"
        "ds_read_b128 v[212:215], %[wl] offset:4096\n\t"
        "ds_read_b128 v[216:219], %[wl] offset:5120\n\t"
        "ds_read_b128 v[220:223], %[wl] offset:6144\n\t"
        "ds_read_b128 v[224:227], %[wl] offset:7168\n\t"
        "ds_read_b128 v[228:231], %[wl] offset:8192\n\t"
        "ds_read_b128 v[232:235], %[wl] offset:9216\n\t"
        "ds_read_b128 v[236:239], %[wl] offset:10240\n\t"
        "ds_read_b128 v[240:243], %[wl] offset:11264\n\t"
        "s_add_u32 s98, s64, %[ep]\n\t"
        "v_mov_b32 v255, s98\n\t"
        "v_and_b32 v253, 0xff, %[l16]\n\t"
        "v_add_u32 v254, %[mw2s], v253\n\t"
        "s_and_b32 s78, s64, s59\n\t"
        "s_cmp_eq_u32 s78, 0\n\t"
        "s_subb_u32 s97, s97, 0\n\t"
        "s_max_u32 s97, s97, 52\n\t"
        "s_lshl_b32 s78, s65, 14\n\t"
        "s_add_u32 s71, %[mp0], s78\n\t"
        "s_add_u32 s72, s71, 4096\n\t"
        "s_lshl_b32 s78, s65, 10\n\t"
        "s_add_u32 s73, %[mp2], s78\n\t"
        "s_mov_b32 s76, 0\n\t"
        "v_add_u32 v252, %[kxw], %[l16]\n\t"
        "s_waitcnt vmcnt(3)\n\t"
        "ds_write_b128 v252, v[132:135]\n\t"
        "ds_write_b128 v252, v[136:139] offset:1024\n\t"
        "s_waitcnt lgkmcnt(0)\n\t"
        "s_barrier\n\t"
        "ds_read_b128 v[132:135], %[l16] offset:13312\n\t"
        "ds_read_b128 v[148:151], %[l16] offset:17408\n\t"
        "ds_read_b128 v[136:139], %[l16] offset:14336\n\t"
        "ds_read_b128 v[152:155], %[l16] offset:18432\n\t"
        "ds_read_b128 v[140:143], %[l16] offset:15360\n\t"
        "ds_read_b128 v[156:159], %[l16] offset:19456\n\t"
        "ds_read_b128 v[144:147], %[l16] offset:16384\n\t"
        "ds_read_b128 v[160:163], %[l16] offset:20480\n\t"
        "v_cmp_eq_u32 vcc, %[ep], v250\n\t"
        "s_add_u32 s78, s64, 1\n\t"
        "s_cmp_lg_u64 vcc, 0\n\t"
        "s_cselect_b32 s94, s78, -1\n\t"
        "v_xor_b32 v250, 1024, %[mw0]\n\t"
        "v_xor_b32 v251, 2048, %[mw0]\n\t"
        "v_xor_b32 v252, 3072, %[mw0]\n\t"
        "s_add_u32 s67, s64, 1\n\t"
        "v_mov_b32 v124, s67\n\t"
        "v_mov_b32 v125, %[flw]\n\t"
        "v_mov_b32 v248, 163776\n\t"
        "s_cmp_lg_u32 s64, 0\n\t"
        "s_cbranch_scc1 HW%=\n\t"
        "ds_read_b128 v[96:99], %[mw0]\n\t"
        "ds_read_b128 v[100:103], %[mw0] offset:4096\n\t"
        "s_waitcnt vmcnt(0) lgkmcnt(0)\n\t"
        "s_branch HGO%=\n\t"
        "HW%=:\n\t"
        "s_mov_b32 s76, s77\n\t"
        "s_sub_u32 s98, s98, 1\n\t"
        "HFL%=:\n\t"
        "v_cmp_ne_u32 vcc, s98, v109\n\t"
        "s_cbranch_vccz HFD%=\n\t"
        "v_cmp_eq_u32 vcc, v255, v109\n\t"
        "s_cbranch_vccnz HPOLL%=\n\t"
        "buffer_load_dwordx2 v[108:109], %[fboff], s[60:63], 0 offen sc1\n\t"
        "s_waitcnt vmcnt(0)\n\t"
        "s_add_u32 s76, s76, 1\n\t"
        "s_cmp_lt_u32 s76, 0x40000\n\t"
        "s_cbranch_scc1 HFL%=\n\t"
        "s_mov_b32 s77, 0x40000\n\t"
        "HFD%=:\n\t"
        "s_memrealtime s[82:83]\n\t"
        "s_memtime s[50:51]\n\t"
        "s_waitcnt lgkmcnt(0)\n\t"
        "s_sub_u32 s78, s82, s52\n\t"
        "s_lshl_b32 s78, s78, 4\n\t"
        "s_lshr_b32 s52, s54, 2\n\t"
        "s_sub_u32 s54, s54, s52\n\t"
        "s_add_u32 s54, s54, s78\n\t"
        "s_sub_u32 s78, s50, s53\n\t"
        "s_lshr_b32 s52, s55, 2\n\t"
        "s_sub_u32 s55, s55, s52\n\t"
        "s_add_u32 s55, s55, s78\n\t"
        "s_mov_b32 s52, s82\n\t"
        "s_mov_b32 s53, s50\n\t"
        "v_cvt_f32_u32 v109, s55\n\t"
        "v_rcp_f32 v109, v109\n\t"
        "v_cvt_f32_u32 v110, s54\n\t"
        "v_mul_f32 v110, 0x430c0000, v110\n\t"
        "v_fma_f32 v109, v109, v110, 0.5\n\t"
        "v_cvt_u32_f32 v109, v109\n\t"
        "v_subrev_u32 v108, s82, v108\n\t"
        "v_add_u32 v108, v108, v109\n\t"
        "s_nop 1\n\t"
        "v_max_i32_dpp v108, v108, v108 row_ror:1 row_mask:0xf bank_mask:0xf\n\t"
        "s_nop 1\n\t"
        "v_max_i32_dpp v108, v108, v108 row_ror:2 row_mask:0xf bank_mask:0xf\n\t"
        "s_nop 1\n\t"
        "v_max_i32_dpp v108, v108, v108 row_ror:4 row_mask:0xf bank_mask:0xf\n\t"
        "s_nop 1\n\t"
        "v_max_i32_dpp v108, v108, v108 row_ror:8 row_mask:0xf bank_mask:0xf\n\t"
        "v_mov_b32 v109, v108\n\t"
        "s_nop 1\n\t"
        "v_permlane16_swap_b32 v108, v109\n\t"
        "v_max_i32 v108, v108, v109\n\t"
        "s_nop 0\n\t"
        "v_readfirstlane_b32 s78, v108\n\t"
        "s_add_u32 s78, s78, s97\n\t"
        "s_min_i32 s78, s78, 1000\n\t"
        "s_add_u32 s96, s82, s78\n\t"
        "HTW%=:\n\t"
        "s_memrealtime s[82:83]\n\t"
        "s_waitcnt lgkmcnt(0)\n\t"
        "s_sub_u32 s78, s82, s96\n\t"
        "s_cmp_lt_i32 s78, 0\n\t"
        "s_cbranch_scc1 HTW%=\n\t"
        "HPOLL%=:\n\t"
        "buffer_load_dwordx4 v[96:99], %[l16], s[60:63], s71 offen sc1\n\t"
        "buffer_load_dwordx4 v[100:103], %[l16], s[60:63], s72 offen sc1\n\t"
        "buffer_load_dwordx4 v[128:131], v253, s[60:63], s73 offen sc1\n\t"
        "s_add_u32 s76, s76, 1\n\t"
        "s_or_b32 s78, s76, s77\n\t"
        "s_waitcnt vmcnt(2)\n\t"
        "ds_write_b128 %[mw0], v[96:99]\n\t"
        "v_max3_u32 v244, v96, v97, v98\n\t"
        "s_waitcnt vmcnt(1)\n\t"
        "ds_write_b128 %[mw0], v[100:103] offset:4096\n\t"
        "v_max3_u32 v244, v244, v99, v100\n\t"
        "v_max3_u32 v245, v101, v102, v103\n\t"
        "s_waitcnt vmcnt(0)\n\t"
        "ds_write_b128 v254, v[128:131]\n\t"
        "v_max3_u32 v246, v128, v129, v130\n\t"
        "v_max3_u32 v244, v244, v245, v131\n\t"
        "v_max_u32 v244, v244, v246\n\t"
        "v_cmp_eq_u32 vcc, -1, v244\n\t"
        "s_cbranch_vccz HGO%=\n\t"
        "s_add_u32 s97, s97, 2\n\t"
        "s_lshl1_add_u32 s59, s59, 1\n\t"
        "s_min_u32 s59, s59, 7\n\t"
        "s_min_u32 s97, s97, 112\n\t"
        "s_cmp_lt_u32 s78, 0x40000\n\t"
        "s_cbranch_scc1 HPOLL%=\n\t"
        "s_mov_b32 s77, 0x40000\n\t"
        "HGO%=:\n\t"
        "ds_write_b32 v125, v124\n\t"
        "v_smfmac_f32_16x16x64_bf16 v[172:175], v[96:99], a[144:151], %[idx]\n\t"
        "v_smfmac_f32_16x16x64_bf16 v[176:179], v[96:99], v[196:203], %[idx]\n\t"
        "v_smfmac_f32_16x16x64_bf16 v[172:175], v[100:103], a[152:159], %[idx]\n\t"
        "ds_read_b128 v[196:199], %[wl] offset:12288\n\t"
        "v_smfmac_f32_16x16x64_bf16 v[176:179], v[100:103], v[204:211], %[idx]\n\t"
        "ds_read_b128 v[200:203], %[wl] offset:13312\n\t"
        "v_smfmac_f32_16x16x64_bf16 v[180:183], v[96:99], a[0:7], %[idx]\n\t"
        "ds_read_b128 v[204:207], %[wl] offset:14336\n\t"
        "v_smfmac_f32_16x16x64_bf16 v[184:187], v[96:99], a[72:79], %[idx]\n\t"
        "ds_read_b128 v[208:211], %[wl] offset:15360\n\t"
        "v_smfmac_f32_16x16x64_bf16 v[180:183], v[100:103], a[8:15], %[idx]\n\t"
        "ds_read_b128 v[244:247], v248\n\t"
        "ds_read_b128 v[104:107], v250\n\t"
        "v_smfmac_f32_16x16x64_bf16 v[184:187], v[100:103], a[80:87], %[idx]\n\t"
        "ds_read_b128 v[108:111], v250 offset:4096\n\t"
        "ds_read_b128 v[112:115], v251\n\t"
        "v_smfmac_f32_16x16x64_bf16 v[188:191], v[96:99], a[192:199], %[idx]\n\t"
        "ds_read_b128 v[116:119], v251 offset:4096\n\t"
        "ds_read_b128 v[120:123], v252\n\t"
        "v_smfmac_f32_16x16x64_bf16 v[192:195], v[96:99], v[24:31], %[idx]\n\t"
        "ds_read_b128 v[124:127], v252 offset:4096\n\t"
        "ds_read_b128 v[128:131], %[l16] offset:8192\n\t"
        "v_smfmac_f32_16x16x64_bf16 v[188:191], v[100:103], a[200:207], %[idx]\n\t"
        "v_smfmac_f32_16x16x64_bf16 v[192:195], v[100:103], v[32:39], %[idx]\n\t"
        "HCHK%=:\n\t"
        "s_waitcnt lgkmcnt(7)\n\t"
        "v_min3_u32 v244, v244, v245, v246\n\t"
        "v_min_u32 v244, v244, v247\n\t"
        "v_cmp_ne_u32 vcc, s67, v244\n\t"
        "s_cbranch_vccz HRUN%=\n\t"
        "ds_read_b128 v[244:247], v248\n\t"
        "ds_read_b128 v[104:107], v250\n\t"
        "ds_read_b128 v[108:111], v250 offset:4096\n\t"
        "ds_read_b128 v[112:115], v251\n\t"
        "ds_read_b128 v[116:119], v251 offset:4096\n\t"
        "ds_read_b128 v[120:123], v252\n\t"
        "ds_read_b128 v[124:127], v252 offset:4096\n\t"
        "ds_read_b128 v[128:131], %[l16] offset:8192\n\t"
        "s_branch HCHK%=\n\t"
        "HRUN%=:\n\t"
        "s_waitcnt lgkmcnt(5)\n\t"
        "v_smfmac_f32_16x16x64_bf16 v[172:175], v[104:107], a[160:167], %[idx]\n\t"
        "buffer_store_dword v255, off, %[RW], %[kmfs] sc1\n\t"
        "v_smfmac_f32_16x16x64_bf16 v[176:179], v[104:107], v[212:219], %[idx]\n\t"
        "s_memrealtime s[98:99]\n\t"
        "v_smfmac_f32_16x16x64_bf16 v[172:175], v[108:111], a[168:175], %[idx]\n\t"
        "s_waitcnt lgkmcnt(0)\n\t"
        "v_smfmac_f32_16x16x64_bf16 v[176:179], v[108:111], v[220:227], %[idx]\n\t"
        "ds_read_b128 v[212:215], %[wl] offset:16384\n\t"
        "v_smfmac_f32_16x16x64_bf16 v[172:175], v[112:115], a[176:183], %[idx]\n\t"
        "ds_read_b128 v[216:219], %[wl] offset:17408\n\t"
        "v_mov_b32 v254, s98\n\t"
        "v_smfmac_f32_16x16x64_bf16 v[176:179], v[112:115], v[228:235], %[idx]\n\t"
        "ds_read_b128 v[220:223], %[wl] offset:18432\n\t"
        "v_smfmac_f32_16x16x64_bf16 v[172:175], v[116:119], a[184:191], %[idx]\n\t"
        "ds_read_b128 v[224:227], %[wl] offset:19456\n\t"
        "v_smfmac_f32_16x16x64_bf16 v[176:179], v[116:119], v[236:243], %[idx]\n\t"
        "ds_read_b128 v[228:231], %[wl] offset:20480\n\t"
        "v_smfmac_f32_16x16x64_bf16 v[172:175], v[120:123], v[196:203], %[idx]\n\t"
        "ds_read_b128 v[232:235], %[wl] offset:21504\n\t"
        "v_smfmac_f32_16x16x64_bf16 v[176:179], v[120:123], v[204:211], %[idx]\n\t"
        "ds_read_b128 v[236:239], %[wl] offset:22528\n\t"
        "v_smfmac_f32_16x16x64_bf16 v[180:183], v[104:107], a[16:23], %[idx]\n\t"
        "ds_read_b128 v[240:243], %[wl] offset:23552\n\t"
        "v_smfmac_f32_16x16x64_bf16 v[184:187], v[104:107], a[88:95], %[idx]\n\t"
        "s_waitcnt lgkmcnt(4)\n\t"
        "v_smfmac_f32_16x16x64_bf16 v[172:175], v[124:127], v[212:219], %[idx]\n\t"
        "buffer_store_dwordx2 v[254:255], off, s[60:63], %[fas] sc1\n\t"
        "v_smfmac_f32_16x16x64_bf16 v[176:179], v[124:127], v[220:227], %[idx]\n\t"
        "s_waitcnt lgkmcnt(2)\n\t"
        "v_smfmac_f32_16x16x64_bf16 v[172:175], v[128:131], v[228:235], %[idx]\n\t"
        "v_smfmac_f32_16x16x64_bf16 v[180:183], v[108:111], a[24:31], %[idx]\n\t"
        "s_waitcnt lgkmcnt(0)\n\t"
        "v_smfmac_f32_16x16x64_bf16 v[176:179], v[128:131], v[236:243], %[idx]\n\t"
        "v_smfmac_f32_16x16x64_bf16 v[184:187], v[108:111], a[96:103], %[idx]\n\t"
        "v_smfmac_f32_16x16x64_bf16 v[180:183], v[112:115], a[32:39], %[idx]\n\t"
        "v_permlane16_swap_b32 v172, v174\n\t"
        "v_smfmac_f32_16x16x64_bf16 v[184:187], v[112:115], a[104:111], %[idx]\n\t"
        "v_permlane16_swap_b32 v173, v175\n\t"
        "v_smfmac_f32_16x16x64_bf16 v[180:183], v[116:119], a[40:47], %[idx]\n\t"
        "v_permlane16_swap_b32 v176, v178\n\t"
        "v_add_f32 v172, v172, v174\n\t"
        "v_smfmac_f32_16x16x64_bf16 v[184:187], v[116:119], a[112:119], %[idx]\n\t"
        "v_permlane16_swap_b32 v177, v179\n\t"
        "v_add_f32 v173, v173, v175\n\t"
        "v_smfmac_f32_16x16x64_bf16 v[180:183], v[120:123], a[48:55], %[idx]\n\t"
        "v_add_f32 v176, v176, v178\n\t"
        "v_add_f32 v177, v177, v179\n\t"
        "v_smfmac_f32_16x16x64_bf16 v[184:187], v[120:123], a[120:127], %[idx]\n\t"
        "v_cvt_pk_bf16_f32 v244, v172, v176\n\t"
        "v_cvt_pk_bf16_f32 v245, v173, v177\n\t"
        "ds_write2_b32 %[qw], v244, v245 offset1:4\n\t"
        "v_smfmac_f32_16x16x64_bf16 v[180:183], v[124:127], a[56:63], %[idx]\n\t"
        "v_smfmac_f32_16x16x64_bf16 v[184:187], v[124:127], a[128:135], %[idx]\n\t"
        "v_smfmac_f32_16x16x64_bf16 v[180:183], v[128:131], a[64:71], %[idx]\n\t"
        "s_waitcnt lgkmcnt(0)\n\t"
        "s_barrier\n\t"
        "v_smfmac_f32_16x16x64_bf16 v[184:187], v[128:131], a[136:143], %[idx]\n\t"
        "ds_read_b128 v[228:231], %[qrd] offset:0\n\t"
        "ds_read_b128 v[232:235], %[qrd] offset:512\n\t"
        "v_smfmac_f32_16x16x64_bf16 v[188:191], v[104:107], a[208:215], %[idx]\n\t"
        "ds_read_b128 v[236:239], %[qrd] offset:1024\n\t"
        "ds_read_b128 v[240:243], %[qrd] offset:1536\n\t"
        "v_smfmac_f32_16x16x64_bf16 v[192:195], v[104:107], v[40:47], %[idx]\n\t"
        "v_smfmac_f32_16x16x64_bf16 v[188:191], v[108:111], a[216:223], %[idx]\n\t"
        "v_permlane16_swap_b32 v180, v182\n\t"
        "s_waitcnt lgkmcnt(2)\n\t"
        "v_mfma_f32_16x16x32_bf16 v[172:175], v[132:135], v[228:231], 0\n\t"
        "v_permlane16_swap_b32 v181, v183\n\t"
        "v_mfma_f32_16x16x32_bf16 v[176:179], v[148:151], v[228:231], 0\n\t"
        "v_permlane16_swap_b32 v184, v186\n\t"
        "v_mfma_f32_16x16x32_bf16 v[172:175], v[136:139], v[232:235], v[172:175]\n\t"
        "v_permlane16_swap_b32 v185, v187\n\t"
        "v_mfma_f32_16x16x32_bf16 v[176:179], v[152:155], v[232:235], v[176:179]\n\t"
        "v_add_f32 v180, v180, v182\n\t"
        "v_add_f32 v181, v181, v183\n\t"
        "s_waitcnt lgkmcnt(0)\n\t"
        "v_mfma_f32_16x16x32_bf16 v[172:175], v[140:143], v[236:239], v[172:175]\n\t"
        "v_add_f32 v184, v184, v186\n\t"
        "v_add_f32 v185, v185, v187\n\t"
        "v_mfma_f32_16x16x32_bf16 v[176:179], v[156:159], v[236:239], v[176:179]\n\t"
        "v_cvt_pk_bf16_f32 v244, v180, v184\n\t"
        "v_cvt_pk_bf16_f32 v245, v181, v185\n\t"
        "v_mfma_f32_16x16x32_bf16 v[172:175], v[144:147], v[240:243], v[172:175]\n\t"
        "ds_write2_b32 %[kw], v244, v245 offset1:4\n\t"
        "v_mfma_f32_16x16x32_bf16 v[176:179], v[160:163], v[240:243], v[176:179]\n\t"
        "v_smfmac_f32_16x16x64_bf16 v[192:195], v[108:111], v[48:55], %[idx]\n\t"
        "v_smfmac_f32_16x16x64_bf16 v[188:191], v[112:115], a[224:231], %[idx]\n\t"
        "s_waitcnt lgkmcnt(0)\n\t"
        "s_barrier\n\t"
        "v_smfmac_f32_16x16x64_bf16 v[192:195], v[112:115], v[56:63], %[idx]\n\t"
        "ds_read_b128 v[196:199], %[krd] offset:0\n\t"
        "ds_read_b128 v[200:203], %[krd] offset:512\n\t"
        "ds_read_b128 v[204:207], %[krd] offset:1024\n\t"
        "ds_read_b128 v[208:211], %[krd] offset:1536\n\t"
        "v_smfmac_f32_16x16x64_bf16 v[188:191], v[116:119], a[232:239], %[idx]\n\t"
        "v_bfi_b32 v246, %[mhi8], v176, v172\n\t"
        "v_bfi_b32 v247, %[mhi8], v177, v173\n\t"
        "v_bfi_b32 v248, %[mhi8], v178, v174\n\t"
        "v_bfi_b32 v249, %[mhi8], v179, v175\n\t"
        "v_smfmac_f32_16x16x64_bf16 v[192:195], v[116:119], v[64:71], %[idx]\n\t"
        "v_max3_f32 v251, v246, v247, v248\n\t"
        "s_waitcnt lgkmcnt(2)\n\t"
        "v_mfma_f32_16x16x32_bf16 v[212:215], v[196:199], v[228:231], 0\n\t"
        "v_max_f32 v251, v251, v249\n\t"
        "v_mul_f32 v246, s95, v246\n\t"
        "v_mfma_f32_16x16x32_bf16 v[212:215], v[200:203], v[232:235], v[212:215]\n\t"
        "s_waitcnt lgkmcnt(0)\n\t"
        "v_max_f32_dpp v251, v251, v251 row_ror:8 row_mask:0xf bank_mask:0xf\n\t"
        "v_mfma_f32_16x16x32_bf16 v[212:215], v[204:207], v[236:239], v[212:215]\n\t"
        "v_mov_b32 v252, v251\n\t"
        "v_mul_f32 v247, s95, v247\n\t"
        "v_mfma_f32_16x16x32_bf16 v[212:215], v[208:211], v[240:243], v[212:215]\n\t"
        "v_permlane16_swap_b32 v251, v252\n\t"
        "v_smfmac_f32_16x16x64_bf16 v[188:191], v[120:123], a[240:247], %[idx]\n\t"
        "v_max_f32 v251, v251, v252\n\t"
        "v_smfmac_f32_16x16x64_bf16 v[192:195], v[120:123], v[72:79], %[idx]\n\t"
        "v_mul_f32 v248, s95, v248\n\t"
        "v_max3_f32 v251, v251, v212, v213\n\t"
        "v_max3_f32 v251, v251, v214, v215\n\t"
        "v_mov_b32 v252, v251\n\t"
        "v_smfmac_f32_16x16x64_bf16 v[188:191], v[124:127], a[248:255], %[idx]\n\t"
        "v_bfi_b32 v172, %[mhi8], v213, v212\n\t"
        "v_permlane32_swap_b32 v251, v252\n\t"
        "v_max_f32 v251, v251, v252\n\t"
        "v_mul_f32 v251, s95, v251\n\t"
        "v_bfi_b32 v173, %[mhi8], v215, v214\n\t"
        "v_sub_f32 v246, v246, v251\n\t"
        "v_sub_f32 v247, v247, v251\n\t"
        "v_exp_f32 v246, v246\n\t"
        "v_smfmac_f32_16x16x64_bf16 v[192:195], v[124:127], v[80:87], %[idx]\n\t"
        "v_exp_f32 v247, v247\n\t"
        "v_mul_f32 v249, s95, v249\n\t"
        "v_sub_f32 v248, v248, v251\n\t"
        "v_bfi_b32 v250, %[mg1], v173, v172\n\t"
        "v_exp_f32 v248, v248\n\t"
        "v_smfmac_f32_16x16x64_bf16 v[188:191], v[128:131], v[16:23], %[idx]\n\t"
        "v_sub_f32 v249, v249, v251\n\t"
        "v_mul_f32 v250, s95, v250\n\t"
        "v_exp_f32 v249, v249\n\t"
        "v_sub_f32 v250, v250, v251\n\t"
        "v_exp_f32 v250, v250\n\t"
        "v_smfmac_f32_16x16x64_bf16 v[192:195], v[128:131], v[88:95], %[idx]\n\t"
        "v_cvt_pk_bf16_f32 v216, v246, v247\n\t"
        "v_cvt_pk_bf16_f32 v217, v248, v249\n\t"
        "v_mov_b32_dpp v253, v250 row_ror:8 row_mask:0xf bank_mask:0xf\n\t"
        "v_mov_b32_dpp v218, v216 row_ror:8 row_mask:0xf bank_mask:0xf\n\t"
        "v_mov_b32_dpp v219, v217 row_ror:8 row_mask:0xf bank_mask:0xf\n\t"
        "v_cvt_pk_bf16_f32 v224, v250, v253\n\t"
        "v_cvt_pk_bf16_f32 v225, v250, v253\n\t"
        "v_mfma_f32_16x16x32_bf16 v[180:183], v[164:167], v[216:219], 0\n\t"
        "v_add_f32 v252, v246, v247\n\t"
        "v_permlane16_swap_b32 v224, v225\n\t"
        "v_mfma_f32_16x16x32_bf16 v[184:187], v[168:171], v[216:219], 0\n\t"
        "v_add_f32 v253, v248, v249\n\t"
        "v_cvt_pk_bf16_f32 v220, v188, v189\n\t"
        "v_cvt_pk_bf16_f32 v221, v190, v191\n\t"
        "v_add_f32 v252, v252, v253\n\t"
        "v_add_f32 v252, v252, v250\n\t"
        "v_mfma_f32_16x16x16_bf16 v[180:183], v[220:221], v[224:225], v[180:183]\n\t"
        "v_cvt_pk_bf16_f32 v222, v192, v193\n\t"
        "v_cvt_pk_bf16_f32 v223, v194, v195\n\t"
        "v_add_f32_dpp v252, v252, v252 row_ror:8 row_mask:0xf bank_mask:0xf\n\t"
        "v_mov_b32 v253, v252\n\t"
        "v_mfma_f32_16x16x16_bf16 v[184:187], v[222:223], v[224:225], v[184:187]\n\t"
        "s_nop 0\n\t"
        "v_permlane16_swap_b32 v252, v253\n\t"
        "v_add_f32 v252, v252, v253\n\t"
        "v_mov_b32 v253, v252\n\t"
        "s_nop 1\n\t"
        "v_permlane32_swap_b32 v252, v253\n\t"
        "v_add_f32 v252, v252, v253\n\t"
        "v_rcp_f32 v252, v252\n\t"
        "s_nop 0\n\t"
        "v_mul_f32 v180, v180, v252\n\t"
        "v_mul_f32 v184, v184, v252\n\t"
        "v_mul_f32 v181, v181, v252\n\t"
        "v_mul_f32 v185, v185, v252\n\t"
        "v_mul_f32 v182, v182, v252\n\t"
        "v_mul_f32 v186, v186, v252\n\t"
        "v_mul_f32 v183, v183, v252\n\t"
        "v_mul_f32 v187, v187, v252\n\t"
        "v_cvt_pk_bf16_f32 v216, v180, v184\n\t"
        "v_cvt_pk_bf16_f32 v217, v181, v185\n\t"
        "v_cvt_pk_bf16_f32 v218, v182, v186\n\t"
        "v_cvt_pk_bf16_f32 v219, v183, v187\n\t"
        "v_min_u32 v216, -2, v216\n\t"
        "v_min_u32 v217, -2, v217\n\t"
        "v_min_u32 v218, -2, v218\n\t"
        "v_min_u32 v219, -2, v219\n\t"
        "buffer_store_dwordx4 v[216:219], %[ooff], s[60:63], s74 offen sc1\n\t"
        "s_add_u32 s66, s66, 0x40000\n\t"
        "s_add_u32 s68, s68, 0x40000\n\t"
        "s_add_u32 s65, s65, 1\n\t"
        "s_cmp_eq_u32 s65, 3\n\t"
        "s_cselect_b32 s65, 0, s65\n\t"
        "s_add_u32 s64, s64, 1\n\t"
        "s_cmp_lt_u32 s64, 64\n\t"
        "s_cbranch_scc1 HLOOP%=\n\t"
        "s_lshl_b32 s78, %[w], 9\n\t"
        "v_add_u32 v244, s78, %[krd]\n\t"
        "ds_read_b128 v[196:199], v244\n\t"
        "v_permlane16_swap_b32 v188, v192\n\t"
        "v_permlane16_swap_b32 v189, v193\n\t"
        "v_permlane16_swap_b32 v190, v194\n\t"
        "v_permlane16_swap_b32 v191, v195\n\t"
        "v_add_f32 v188, v188, v192\n\t"
        "v_add_f32 v189, v189, v193\n\t"
        "v_add_f32 v190, v190, v194\n\t"
        "v_add_f32 v191, v191, v195\n\t"
        "v_cvt_pk_bf16_f32 v220, v188, v189\n\t"
        "v_cvt_pk_bf16_f32 v221, v190, v191\n\t"
        "buffer_store_dwordx2 v[220:221], %[vmoff], %[RW], s70 offen sc1\n\t"
        "s_waitcnt lgkmcnt(0)\n\t"
        "buffer_store_dwordx4 v[196:199], %[kmoff], %[RW], s69 offen sc1\n\t"
        "s_waitcnt vmcnt(0)\n\t"
        "s_barrier\n\t"
        "s_add_u32 s78, %[ep], 64\n\t"
        "v_mov_b32 v255, s78\n\t"
        "buffer_store_dword v255, off, %[RW], %[kmfs] sc1\n\t"
        "s_mov_b32 %[dead], s77\n\t"
        : [dead] "=s"(dead)
        : [idx] "v"(idx), [l16] "v"(l16), [wl] "v"(wl), [qw] "v"(qw), [kw] "v"(kw), [qrd] "v"(qrd), [krd] "v"(krd), [ooff] "v"(ooff), [vmoff] "v"(vmoff), [kmoff] "v"(kmoff), [mhi8] "v"(mhi8), [mg1] "v"(mg1), [mw0] "v"(mw0), [fboff] "v"(fboff), [W0] "s"(W0), [W1] "s"(W1), [W2] "s"(W2), [comm] "s"(comm), [w] "s"(w), [RW] "s"(RW), [kx0] "s"(kx0), [vx0] "s"(vx0), [km0] "s"(km0), [vm0] "s"(vm0), [mp0] "s"(mp0), [mp2] "s"(mp2), [fas] "s"(fas), [kmfs] "s"(kmfs), [kvfs] "s"(kvfs), [mw2s] "s"(mw2s), [kxw] "s"(kxw), [flw] "s"(flw), [ep] "s"(ep), [ords] "s"(ords)
        : "memory", "vcc", "scc", "v16", "v17", "v18", "v19", "v20", "v21", "v22", "v23", "v24", "v25", "v26", "v27", "v28", "v29", "v30", "v31", "v32", "v33", "v34", "v35", "v36", "v37", "v38", "v39", "v40", "v41", "v42", "v43", "v44", "v45", "v46", "v47", "v48", "v49", "v50", "v51", "v52", "v53", "v54", "v55", "v56", "v57", "v58", "v59", "v60", "v61", "v62", "v63", "v64", "v65", "v66", "v67", "v68", "v69", "v70", "v71", "v72", "v73", "v74", "v75", "v76", "v77", "v78", "v79", "v80", "v81", "v82", "v83", "v84", "v85", "v86", "v87", "v88", "v89", "v90", "v91", "v92", "v93", "v94", "v95", "v96", "v97", "v98", "v99", "v100", "v101", "v102", "v103", "v104", "v105", "v106", "v107", "v108", "v109", "v110", "v111", "v112", "v113", "v114", "v115", "v116", "v117", "v118", "v119", "v120", "v121", "v122", "v123", "v124", "v125", "v126", "v127", "v128", "v129", "v130", "v131", "v132", "v133", "v134", "v135", "v136", "v137", "v138", "v139", "v140", "v141", "v142", "v143", "v144", "v145", "v146", "v147", "v148", "v149", "v150", "v151", "v152", "v153", "v154", "v155", "v156", "v157", "v158", "v159", "v160", "v161", "v162", "v163", "v164", "v165", "v166", "v167", "v168", "v169", "v170", "v171", "v172", "v173", "v174", "v175", "v176", "v177", "v178", "v179", "v180", "v181", "v182", "v183", "v184", "v185", "v186", "v187", "v188", "v189", "v190", "v191", "v192", "v193", "v194", "v195", "v196", "v197", "v198", "v199", "v200", "v201", "v202", "v203", "v204", "v205", "v206", "v207", "v208", "v209", "v210", "v211", "v212", "v213", "v214", "v215", "v216", "v217", "v218", "v219", "v220", "v221", "v222", "v223", "v224", "v225", "v226", "v227", "v228", "v229", "v230", "v231", "v232", "v233", "v234", "v235", "v236", "v237", "v238", "v239", "v240", "v241", "v242", "v243", "v244", "v245", "v246", "v247", "v248", "v249", "v250", "v251", "v252", "v253", "v254", "v255", "a0", "a1", "a2", "a3", "a4", "a5", "a6", "a7", "a8", "a9", "a10", "a11", "a12", "a13", "a14", "a15", "a16", "a17", "a18", "a19", "a20", "a21", "a22", "a23", "a24", "a25", "a26", "a27", "a28", "a29", "a30", "a31", "a32", "a33", "a34", "a35", "a36", "a37", "a38", "a39", "a40", "a41", "a42", "a43", "a44", "a45", "a46", "a47", "a48", "a49", "a50", "a51", "a52", "a53", "a54", "a55", "a56", "a57", "a58", "a59", "a60", "a61", "a62", "a63", "a64", "a65", "a66", "a67", "a68", "a69", "a70", "a71", "a72", "a73", "a74", "a75", "a76", "a77", "a78", "a79", "a80", "a81", "a82", "a83", "a84", "a85", "a86", "a87", "a88", "a89", "a90", "a91", "a92", "a93", "a94", "a95", "a96", "a97", "a98", "a99", "a100", "a101", "a102", "a103", "a104", "a105", "a106", "a107", "a108", "a109", "a110", "a111", "a112", "a113", "a114", "a115", "a116", "a117", "a118", "a119", "a120", "a121", "a122", "a123", "a124", "a125", "a126", "a127", "a128", "a129", "a130", "a131", "a132", "a133", "a134", "a135", "a136", "a137", "a138", "a139", "a140", "a141", "a142", "a143", "a144", "a145", "a146", "a147", "a148", "a149", "a150", "a151", "a152", "a153", "a154", "a155", "a156", "a157", "a158", "a159", "a160", "a161", "a162", "a163", "a164", "a165", "a166", "a167", "a168", "a169", "a170", "a171", "a172", "a173", "a174", "a175", "a176", "a177", "a178", "a179", "a180", "a181", "a182", "a183", "a184", "a185", "a186", "a187", "a188", "a189", "a190", "a191", "a192", "a193", "a194", "a195", "a196", "a197", "a198", "a199", "a200", "a201", "a202", "a203", "a204", "a205", "a206", "a207", "a208", "a209", "a210", "a211", "a212", "a213", "a214", "a215", "a216", "a217", "a218", "a219", "a220", "a221", "a222", "a223", "a224", "a225", "a226", "a227", "a228", "a229", "a230", "a231", "a232", "a233", "a234", "a235", "a236", "a237", "a238", "a239", "a240", "a241", "a242", "a243", "a244", "a245", "a246", "a247", "a248", "a249", "a250", "a251", "a252", "a253", "a254", "a255", "s50", "s51", "s52", "s53", "s54", "s55", "s56", "s57", "s58", "s59", "s60", "s61", "s62", "s63", "s64", "s65", "s66", "s67", "s68", "s69", "s70", "s71", "s72", "s73", "s74", "s75", "s76", "s77", "s78", "s79", "s80", "s81", "s82", "s83", "s84", "s85", "s86", "s87", "s88", "s89", "s90", "s91", "s92", "s93", "s94", "s95", "s96", "s97", "s98", "s99");

    if (dead) set_tmo(ctl, ep);
}

__device__ __forceinline__ void o_role(const Args& a, int rb, LAS char* lds, u32 ep) {
    const int tid = threadIdx.x, w = __builtin_amdgcn_readfirstlane(tid >> 6), l = tid & 63, cl = l & 15, g = l >> 4;
    char* ws = a.ws;
    gu32* ctl = (gu32*)(ws + WS_CTL);
    const int ct = rb >> 1, th = rb & 1;
    const int rot = 0;
    const u32 mline = UNI(rb < 64 ? kbo(KB_MEM) + rb * 128 : kbo(KB_MEM8) + (rb - 64) * 128), mstride = UNI(rb < 64 ? 16384 : 1024);
    int phase;
    u32x8 wo[8][2];
    {
        LAS char* st = lds + 4096 + w * 32768;
        if (w == 0) {
            const rsrc_t Ri = __builtin_amdgcn_make_buffer_rsrc(ws + WS_COMM, 0, 0x7fffffff, 0x00020000);
            bst4(Ri, (l & 31) * 4 + (1 + (l >> 5)) * mstride, mline, ~0u);
            bst4(Ri, (l & 31) * 4 + (1 + (l >> 5)) * mstride, mline + 8192, ~0u);
        }
#pragma unroll
        for (int i = 0; i < 32; ++i) {
            dma16(a.Wo + (size_t)(w * 1024 + i * 32 + (l >> 1)) * D + 16 * ct + (l & 1) * 8, st + i * 1024);
            if (i == 3 && w == 0) { VMWAIT(4); if (l == 0) __hip_atomic_store(ctl + C_ORDY + rb, ep, RLX_AGENT); }
        }
        const int q = cl >> 2;
        const LAS char* rp = st + (16 * (q & 1) + 4 * (q >> 1) + 8 * (g >> 1) + 2 * (g & 1)) * 32 + (cl & 3) * 8;
        phase = wait_phase(ctl, ep);
        VMWAIT(0);
#pragma unroll
        for (int i = 0; i < 8; ++i)
#pragma unroll
            for (int ks = 0; ks < 2; ++ks) {
                u32x8 t;
#pragma unroll
                for (int k = 0; k < 4; ++k) { const u32x2 x = trread(rp + (128 * (i ^ rot) + 64 * ks + 32 * (k >> 1) + (k & 1)) * 32); t[2 * k] = x[0]; t[2 * k + 1] = x[1]; }
                wo[i][ks] = toV(t);
            }
    }
    const u64 rb_ = (u64)(ws + WS_COMM + phase * 8192);
    const u32x4 R = {(u32)rb_, (u32)(rb_ >> 32), 0x7fffffffu, 0x00020000u};
    const int idx = ((cl >> 2) & 1) ? 0xEEEE : 0x4444;
    const u32 l16 = l * 16;
    const u32 lw = ((cl >> 3) * 32 + g * 8 + (cl & 7)) * 16 + w * 4;
    const u32 foff = lineoff(KB_FA, 8 * w + (l & 7));
    const u32 mh32 = l >= 32 ? ~0u : 0u, lrot = l16 + rot * 1024;
    const u32 mo = (l & 1) ? OOB : l * 2;
    const u32 ob0 = UNI((th * 4 + w) * 16384);
    u32 dead;
    asm volatile(
        "s_mov_b32 s75, 13\n\t"
        "s_mov_b32 s76, 0\n\t"
        "s_mov_b32 s64, 0\n\t"
        "s_mov_b32 s65, 0\n\t"
        "s_mov_b32 s71, 0\n\t"
        "v_mov_b32 v215, -1\n\t"
        "v_xor_b32 v216, 0, %[lrot]\n\t"
        "v_xor_b32 v217, 1024, %[lrot]\n\t"
        "v_xor_b32 v218, 2048, %[lrot]\n\t"
        "v_xor_b32 v219, 3072, %[lrot]\n\t"
        "v_xor_b32 v220, 4096, %[lrot]\n\t"
        "v_xor_b32 v221, 5120, %[lrot]\n\t"
        "v_xor_b32 v222, 6144, %[lrot]\n\t"
        "v_xor_b32 v223, 7168, %[lrot]\n\t"
        "s_memrealtime s[82:83]\n\t"
        "s_memtime s[58:59]\n\t"
        "s_waitcnt lgkmcnt(0)\n\t"
        "s_mov_b32 s60, s82\n\t"
        "s_mov_b32 s61, s58\n\t"
        "s_mov_b32 s62, 6400\n\t"
        "s_mov_b32 s63, 9060\n\t"
        "OLOOP%=:\n\t"
        "s_lshl_b32 s72, s65, 17\n\t"
        "s_add_u32 s66, %[ob0], s72\n\t"
        "s_mul_i32 s72, s65, %[mstride]\n\t"
        "s_add_u32 s69, %[mline], s72\n\t"
        "s_add_u32 s65, s65, 1\n\t"
        "s_cmp_eq_u32 s65, 3\n\t"
        "s_cselect_b32 s65, 0, s65\n\t"
        "s_mul_i32 s72, s65, %[mstride]\n\t"
        "s_add_u32 s68, %[mline], s72\n\t"
        "s_and_b32 s72, s64, 1\n\t"
        "s_lshl_b32 s73, s72, 10\n\t"
        "v_add_u32 v212, s73, %[lw]\n\t"
        "v_add_u32 v213, s73, %[l16]\n\t"
        "s_mov_b32 s70, s71\n\t"
        "s_and_b32 s72, s64, s76\n\t"
        "s_cmp_eq_u32 s72, 0\n\t"
        "s_subb_u32 s75, s75, 0\n\t"
        "s_max_u32 s75, s75, 13\n\t"
        "OFL%=:\n\t"
        "buffer_load_dwordx2 v[200:201], %[foff], %[R], 0 offen sc1\n\t"
        "s_add_u32 s72, s64, %[ep]\n\t"
        "s_waitcnt vmcnt(0)\n\t"
        "v_cmp_ne_u32 vcc, s72, v201\n\t"
        "s_cbranch_vccz OFD%=\n\t"
        "s_add_u32 s70, s70, 1\n\t"
        "s_cmp_lt_u32 s70, 0x80000\n\t"
        "s_cbranch_scc1 OFL%=\n\t"
        "s_mov_b32 s71, 0x80000\n\t"
        "s_mov_b32 s70, 0x80000\n\t"
        "OFD%=:\n\t"
        "s_memrealtime s[82:83]\n\t"
        "s_memtime s[58:59]\n\t"
        "s_waitcnt lgkmcnt(0)\n\t"
        "s_sub_u32 s72, s82, s60\n\t"
        "s_lshl_b32 s72, s72, 4\n\t"
        "s_lshr_b32 s60, s62, 2\n\t"
        "s_sub_u32 s62, s62, s60\n\t"
        "s_add_u32 s62, s62, s72\n\t"
        "s_sub_u32 s72, s58, s61\n\t"
        "s_lshr_b32 s60, s63, 2\n\t"
        "s_sub_u32 s63, s63, s60\n\t"
        "s_add_u32 s63, s63, s72\n\t"
        "s_mov_b32 s60, s82\n\t"
        "s_mov_b32 s61, s58\n\t"
        "v_cvt_f32_u32 v201, s63\n\t"
        "v_rcp_f32 v201, v201\n\t"
        "v_cvt_f32_u32 v202, s62\n\t"
        "v_mul_f32 v202, 0x42a78000, v202\n\t"
        "v_fma_f32 v201, v201, v202, 0.5\n\t"
        "v_cvt_u32_f32 v201, v201\n\t"
        "v_subrev_u32 v200, s82, v200\n\t"
        "v_add_u32 v200, v200, v201\n\t"
        "s_nop 1\n\t"
        "v_max_i32_dpp v200, v200, v200 row_ror:1 row_mask:0xf bank_mask:0xf\n\t"
        "s_nop 1\n\t"
        "v_max_i32_dpp v200, v200, v200 row_ror:2 row_mask:0xf bank_mask:0xf\n\t"
        "s_nop 1\n\t"
        "v_max_i32_dpp v200, v200, v200 row_ror:4 row_mask:0xf bank_mask:0xf\n\t"
        "s_nop 0\n\t"
        "v_readfirstlane_b32 s72, v200\n\t"
        "s_add_u32 s72, s72, s75\n\t"
        "s_min_i32 s72, s72, 1000\n\t"
        "s_add_u32 s74, s82, s72\n\t"
        "OTW%=:\n\t"
        "s_memrealtime s[82:83]\n\t"
        "s_waitcnt lgkmcnt(0)\n\t"
        "s_sub_u32 s72, s82, s74\n\t"
        "s_cmp_lt_i32 s72, 0\n\t"
        "s_cbranch_scc1 OTW%=\n\t"
        "ODATA%=:\n\t"
        "ORD%=:\n\t"
        "buffer_load_dwordx4 v[160:163], v216, %[R], s66 offen sc1\n\t"
        "buffer_load_dwordx4 v[164:167], v217, %[R], s66 offen sc1\n\t"
        "buffer_load_dwordx4 v[168:171], v218, %[R], s66 offen sc1\n\t"
        "buffer_load_dwordx4 v[172:175], v219, %[R], s66 offen sc1\n\t"
        "buffer_load_dwordx4 v[176:179], v220, %[R], s66 offen sc1\n\t"
        "buffer_load_dwordx4 v[180:183], v221, %[R], s66 offen sc1\n\t"
        "buffer_load_dwordx4 v[184:187], v222, %[R], s66 offen sc1\n\t"
        "buffer_load_dwordx4 v[188:191], v223, %[R], s66 offen sc1\n\t"
        "v_mov_b32 v192, 0\n\t"
        "v_mov_b32 v193, 0\n\t"
        "v_mov_b32 v194, 0\n\t"
        "v_mov_b32 v195, 0\n\t"
        "v_mov_b32 v196, 0\n\t"
        "v_mov_b32 v197, 0\n\t"
        "v_mov_b32 v198, 0\n\t"
        "v_mov_b32 v199, 0\n\t"
        "s_waitcnt vmcnt(7)\n\t"
        "v_smfmac_f32_16x16x64_bf16 v[192:195], v[160:163], %[w00], %[idx]\n\t"
        "v_max3_u32 v204, v160, v161, v162\n\t"
        "v_smfmac_f32_16x16x64_bf16 v[196:199], v[160:163], %[w01], %[idx]\n\t"
        "v_max_u32 v204, v204, v163\n\t"
        "s_waitcnt vmcnt(6)\n\t"
        "v_smfmac_f32_16x16x64_bf16 v[192:195], v[164:167], %[w10], %[idx]\n\t"
        "v_max3_u32 v204, v204, v164, v165\n\t"
        "v_smfmac_f32_16x16x64_bf16 v[196:199], v[164:167], %[w11], %[idx]\n\t"
        "v_max3_u32 v204, v204, v166, v167\n\t"
        "s_waitcnt vmcnt(5)\n\t"
        "v_smfmac_f32_16x16x64_bf16 v[192:195], v[168:171], %[w20], %[idx]\n\t"
        "v_max3_u32 v204, v204, v168, v169\n\t"
        "v_smfmac_f32_16x16x64_bf16 v[196:199], v[168:171], %[w21], %[idx]\n\t"
        "v_max3_u32 v204, v204, v170, v171\n\t"
        "s_waitcnt vmcnt(4)\n\t"
        "v_smfmac_f32_16x16x64_bf16 v[192:195], v[172:175], %[w30], %[idx]\n\t"
        "v_max3_u32 v204, v204, v172, v173\n\t"
        "v_smfmac_f32_16x16x64_bf16 v[196:199], v[172:175], %[w31], %[idx]\n\t"
        "v_max3_u32 v204, v204, v174, v175\n\t"
        "s_waitcnt vmcnt(3)\n\t"
        "v_smfmac_f32_16x16x64_bf16 v[192:195], v[176:179], %[w40], %[idx]\n\t"
        "v_max3_u32 v204, v204, v176, v177\n\t"
        "v_smfmac_f32_16x16x64_bf16 v[196:199], v[176:179], %[w41], %[idx]\n\t"
        "v_max3_u32 v204, v204, v178, v179\n\t"
        "s_waitcnt vmcnt(2)\n\t"
        "v_smfmac_f32_16x16x64_bf16 v[192:195], v[180:183], %[w50], %[idx]\n\t"
        "v_max3_u32 v204, v204, v180, v181\n\t"
        "v_smfmac_f32_16x16x64_bf16 v[196:199], v[180:183], %[w51], %[idx]\n\t"
        "v_max3_u32 v204, v204, v182, v183\n\t"
        "s_waitcnt vmcnt(1)\n\t"
        "v_smfmac_f32_16x16x64_bf16 v[192:195], v[184:187], %[w60], %[idx]\n\t"
        "v_max3_u32 v204, v204, v184, v185\n\t"
        "v_smfmac_f32_16x16x64_bf16 v[196:199], v[184:187], %[w61], %[idx]\n\t"
        "v_max3_u32 v204, v204, v186, v187\n\t"
        "s_waitcnt vmcnt(0)\n\t"
        "v_smfmac_f32_16x16x64_bf16 v[192:195], v[188:191], %[w70], %[idx]\n\t"
        "v_max3_u32 v204, v204, v188, v189\n\t"
        "v_smfmac_f32_16x16x64_bf16 v[196:199], v[188:191], %[w71], %[idx]\n\t"
        "v_max3_u32 v204, v204, v190, v191\n\t"
        "v_cmp_eq_u32 vcc, -1, v204\n\t"
        "s_cbranch_vccz OOK%=\n\t"
        "s_add_u32 s75, s75, 2\n\t"
        "s_lshl1_add_u32 s76, s76, 1\n\t"
        "s_min_u32 s76, s76, 7\n\t"
        "s_min_u32 s75, s75, 53\n\t"
        "s_add_u32 s70, s70, 1\n\t"
        "s_cmp_lt_u32 s70, 0x80000\n\t"
        "s_cbranch_scc1 ORD%=\n\t"
        "s_mov_b32 s71, 0x80000\n\t"
        "OOK%=:\n\t"
        "s_nop 3\n\t"
        "v_bfi_b32 v204, %[mh32], v196, v192\n\t"
        "v_bfi_b32 v205, %[mh32], v197, v193\n\t"
        "v_bfi_b32 v206, %[mh32], v198, v194\n\t"
        "v_bfi_b32 v207, %[mh32], v199, v195\n\t"
        "s_nop 0\n\t"
        "v_permlane32_swap_b32 v204, v206\n\t"
        "v_permlane32_swap_b32 v205, v207\n\t"
        "v_add_f32 v204, v204, v206\n\t"
        "v_add_f32 v205, v205, v207\n\t"
        "s_nop 1\n\t"
        "v_permlane16_swap_b32 v204, v205\n\t"
        "v_add_f32 v204, v204, v205\n\t"
        "ds_write_b32 v212, v204\n\t"
        "s_add_u32 s64, s64, 1\n\t"
        "s_cmp_lg_u32 %[w], 0\n\t"
        "s_waitcnt lgkmcnt(0)\n\t"
        "s_barrier\n\t"
        "s_cbranch_scc1 ONEXT%=\n\t"
        "ds_read_b128 v[208:211], v213\n\t"
        "s_waitcnt lgkmcnt(0)\n\t"
        "v_add_f32 v208, v208, v209\n\t"
        "v_add_f32 v210, v210, v211\n\t"
        "v_add_f32 v208, v208, v210\n\t"
        "s_nop 1\n\t"
        "v_mov_b32_dpp v209, v208 quad_perm:[1,0,3,2] row_mask:0xf bank_mask:0xf\n\t"
        "v_cvt_pk_bf16_f32 v208, v208, v209\n\t"
        "v_min_u32 v208, -2, v208\n\t"
        "buffer_store_dword v208, %[mo], %[R], s68 offen sc1\n\t"
        "buffer_store_dword v215, %[mo], %[R], s69 offen sc1\n\t"
        "ONEXT%=:\n\t"
        "s_cmp_lt_u32 s64, 63\n\t"
        "s_cbranch_scc1 OLOOP%=\n\t"
        "s_waitcnt vmcnt(0)\n\t"
        "s_mov_b32 %[dead], s71\n\t"
        : [dead] "=s"(dead)
        : [w00] "v"(wo[0][0]), [w01] "v"(wo[0][1]), [w10] "v"(wo[1][0]), [w11] "v"(wo[1][1]), [w20] "v"(wo[2][0]), [w21] "v"(wo[2][1]), [w30] "v"(wo[3][0]), [w31] "v"(wo[3][1]), [w40] "v"(wo[4][0]), [w41] "v"(wo[4][1]), [w50] "v"(wo[5][0]), [w51] "v"(wo[5][1]), [w60] "v"(wo[6][0]), [w61] "v"(wo[6][1]), [w70] "v"(wo[7][0]), [w71] "v"(wo[7][1]), [idx] "v"(idx), [l16] "v"(l16), [lw] "v"(lw), [foff] "v"(foff), [mh32] "v"(mh32), [mo] "v"(mo), [lrot] "v"(lrot), [R] "s"(R), [ob0] "s"(ob0), [mstride] "s"(mstride), [mline] "s"(mline), [w] "s"(w), [ep] "s"(ep)
        : "memory", "vcc", "scc", "v160", "v161", "v162", "v163", "v164", "v165", "v166", "v167", "v168", "v169", "v170", "v171", "v172", "v173", "v174", "v175", "v176", "v177", "v178", "v179", "v180", "v181", "v182", "v183", "v184", "v185", "v186", "v187", "v188", "v189", "v190", "v191", "v192", "v193", "v194", "v195", "v196", "v197", "v198", "v199", "v200", "v201", "v202", "v203", "v204", "v205", "v206", "v207", "v208", "v209", "v210", "v211", "v212", "v213", "v214", "v215", "v216", "v217", "v218", "v219", "v220", "v221", "v222", "v223", "s58", "s59", "s60", "s61", "s62", "s63", "s64", "s65", "s66", "s67", "s68", "s69", "s70", "s71", "s72", "s73", "s74", "s75", "s76", "s77", "s78", "s79", "s80", "s81", "s82", "s83", "s84", "s85", "s86", "s87", "s88", "s89", "s90", "s91");

    if (dead) set_tmo(ctl, ep);
}

template <bool VTYPE>
__device__ __forceinline__ void qkv_seg(const u32x4 (&wf)[4][18], const LAS char* xb, int l, rsrc_t RW, u32 sbase, int w) {
    f32x4 acc[4][2];
#pragma unroll
    for (int c = 0; c < 4; ++c) { acc[c][0] = f32x4{0, 0, 0, 0}; acc[c][1] = f32x4{0, 0, 0, 0}; }
    u32x4 xa[3], xc[3];
#pragma unroll
    for (int i = 0; i < 2; ++i) { xa[i] = *(const LAS u32x4*)(xb + i * 1024 + l * 16); xc[i] = *(const LAS u32x4*)(xb + (18 + i) * 1024 + l * 16); }
#pragma unroll
    for (int ks = 0; ks < 18; ++ks) {
        if (ks + 2 < 18) { xa[(ks + 2) % 3] = *(const LAS u32x4*)(xb + (ks + 2) * 1024 + l * 16); xc[(ks + 2) % 3] = *(const LAS u32x4*)(xb + (20 + ks) * 1024 + l * 16); }
        SB();
        const u32x4 x0 = xa[ks % 3], x1 = xc[ks % 3];
#pragma unroll
        for (int c = 0; c < 4; ++c) {
            if (VTYPE) { acc[c][0] = mf32(x0, wf[c][ks], acc[c][0]); acc[c][1] = mf32(x1, wf[c][ks], acc[c][1]); }
            else { acc[c][0] = mf32(wf[c][ks], x0, acc[c][0]); acc[c][1] = mf32(wf[c][ks], x1, acc[c][1]); }
        }
        SB();
    }
    if (VTYPE) {
#pragma unroll
        for (int c = 0; c < 4; ++c)
            bst16(RW, l * 16, sbase + (4 * (w & 1) + c) * 1024,
                  u32x4{pk2(acc[c][0][0], acc[c][0][1]), pk2(acc[c][0][2], acc[c][0][3]), pk2(acc[c][1][0], acc[c][1][1]), pk2(acc[c][1][2], acc[c][1][3])});
    } else {
#pragma unroll
        for (int k2 = 0; k2 < 2; ++k2)
#pragma unroll
            for (int tt = 0; tt < 2; ++tt)
                bst16(RW, l * 16, sbase + tt * 4096 + (2 * (w & 1) + k2) * 1024,
                      u32x4{pk2(acc[2 * k2][tt][0], acc[2 * k2 + 1][tt][0]), pk2(acc[2 * k2][tt][1], acc[2 * k2 + 1][tt][1]),
                            pk2(acc[2 * k2][tt][2], acc[2 * k2 + 1][tt][2]), pk2(acc[2 * k2][tt][3], acc[2 * k2 + 1][tt][3])});
    }
}

__device__ __forceinline__ void qkv_phase(const Args& a, int wid, LAS char* lds, rsrc_t RW, u32 ep) {
    const int tid = threadIdx.x, w = __builtin_amdgcn_readfirstlane(tid >> 6), l = tid & 63, cl = l & 15, g = l >> 4;
    const bool isKV = wid < 96;
    const int grp = isKV ? wid >> 5 : (wid - 96) >> 4;
    const int h0 = isKV ? (wid & 31) : 2 * ((wid - 96) & 15), h1 = isKV ? h0 : h0 + 1;
    const u16* W0 = (isKV ? a.Wk : a.Wq) + h0 * DH;
    const u16* W1 = (isKV ? a.Wv : a.Wq) + h1 * DH;
    const u16* xs = a.u + (size_t)cl * D + 8 * g;
    auto xissue = [&](int s, int buf) {
#pragma unroll
        for (int i = 0; i < 9; ++i) {
            const int p = w * 9 + i, tile = p >= 18, ks = p - 18 * tile;
            dma16(xs + (size_t)(s * SEG + 16 * tile) * D + 32 * ks, lds + LDS_STG + buf * 49152 + p * 1024);
        }
    };
    u32x4 wf[4][18];
    {
        auto issue = [&](int c) {
#pragma unroll
            for (int i = 0; i < 12; ++i) {
                const int row = ((w & 1) * 12 + i) * 4 + g;
                const int f = 2 * ((row & 3) | ((row >> 1) & 4));
                dma16((w >> 1 ? W1 : W0) + (size_t)(c * 96 + row) * HD + ((cl ^ f) * 8), lds + LDS_STG + (c % 3) * 49152 + (w * 12 + i) * 1024);
            }
        };
        issue(0); issue(1);
        const int rq = 8 * g + (cl >> 2);
        const int fq = 2 * ((rq & 3) | ((rq >> 1) & 4));
#pragma unroll
        for (int c = 0; c < 6; ++c) {
            if (c < 5) VMWAIT(12); else VMWAIT(9);
            BAR();
            if (c + 2 < 6) issue(c + 2); else xissue(grp + 3 * (c - 4), c - 4);
#pragma unroll
            for (int k3 = 0; k3 < 3; ++k3)
#pragma unroll
                for (int ct = 0; ct < 4; ++ct) {
                    const int chunk = (8 * (w & 1) + 2 * ct + ((cl & 3) >> 1)) ^ fq;
                    const LAS char* p = lds + LDS_STG + (c % 3) * 49152 + ((w >> 1) * 96 + k3 * 32 + rq) * 256 + chunk * 16 + (cl & 1) * 8;
                    const u32x2 x = trread(p), y = trread(p + 4 * 256);
                    const u32x4 fr = {x[0], x[1], y[0], y[1]};
                    const int ks = c * 3 + k3;
                    wf[ct][ks] = (ks < 14) ? toA4(fr) : toV4(fr);
                }
            SB();
        }
        VMWAIT(9);
        BAR();
    }
    const int hm = (w >> 1) ? h1 : h0;
    const u32 obase = isKV ? ((w >> 1) ? WS_VXF : WS_KXF) : WS_QXF;
    const u32 fword = isKV ? (tid == 0 ? (u32)(C_KVF + h0) * 4 : OOB) : (tid < 2 ? (u32)(C_QF + h0 + tid) * 4 : OOB);
    int buf = 0, slast = 0;
    for (int s = grp; s < NSEG; s += 3) {
        const bool more = s + 6 < NSEG;
        if (more && s >= 3) xissue(s + 6, buf == 0 ? 2 : buf - 1);
        SB();
        const u32 sbase = UNI(obase + (u32)(s * H + hm) * 8192);
        if (isKV && (w >> 1)) qkv_seg<true>(wf, lds + LDS_STG + buf * 49152, l, RW, sbase, w);
        else qkv_seg<false>(wf, lds + LDS_STG + buf * 49152, l, RW, sbase, w);
        SB();
        if (s < 3) VMWAIT(0); else if (more) VMWAIT(13); else VMWAIT(4);
        BAR();
        if (w == 0 && (s < 3 || s >= 6)) bst4(RW, fword, WS_CTL + (s < 3 ? s : s - 3) * 128, ep);
        if (s < 3) xissue(s + 6, 2);
        buf = buf == 2 ? 0 : buf + 1;
        slast = s;
    }
    VMWAIT(0);
    BAR();
    if (w == 0) bst4(RW, fword, WS_CTL + slast * 128, ep);
}

__device__ __forceinline__ void calibrate(const Args& a, LAS char* lds, u32 ep) {
    const int w = threadIdx.x >> 6;
    unsigned b0 = ~0u, b1 = ~0u;
    for (int r = 0; r < 8; ++r) {
        char* p = a.ws + WS_COMM + (r & 1) * 8192 + (w & 1) * 4096 + (size_t)((r >> 1) * 2 + (w >> 1)) * 16384 + r * 128;
        const u64 t0 = clk();
        asm volatile("global_store_dword %0, %1, off sc1\n\ts_waitcnt vmcnt(0)" :: "v"(p), "v"(~0u) : "memory");
        const unsigned dt = (unsigned)(clk() - t0);
        if (r & 1) b1 = min(b1, dt); else b0 = min(b0, dt);
    }
    if ((threadIdx.x & 63) == 0) { ((LAS u32*)lds)[w * 2] = b0; ((LAS u32*)lds)[w * 2 + 1] = b1; }
    BAR();
    if (threadIdx.x == 0) {
        LAS u32* t = (LAS u32*)lds;
        const u32 s0 = min(min(t[0], t[2]), min(t[4], t[6])), s1 = min(min(t[1], t[3]), min(t[5], t[7]));
        t[8] = s0; t[9] = s1;
        __hip_atomic_store((gu32*)(a.ws + WS_CTL) + C_PHASE, ep + (s1 < s0 ? 1u : 0u), RLX_AGENT);
    }
}

#define SE 0
#define WARM 1
__device__ __forceinline__ void gate_phase(const Args& a, int gi, rsrc_t RW, u32 ep) {
    const int tid = threadIdx.x, sub = tid & 7, w = __builtin_amdgcn_readfirstlane(tid >> 6), l = tid & 63;
    float sm[8];
#pragma unroll
    for (int p = 0; p < 8; ++p) {
        const int t = gi * 256 + p * 32 + (tid >> 3);
        sm[p] = 0.f;
#pragma unroll
        for (int i = 0; i < 9; ++i) {
            const u32x4 x = *(const u32x4*)(a.u + (size_t)t * D + (i * 8 + sub) * 8), gw = *(const u32x4*)(a.gw + (i * 8 + sub) * 8);
#pragma unroll
            for (int k = 0; k < 4; ++k) { const u32 pr = pk2(lo_f(x[k]) * lo_f(gw[k]), hi_f(x[k]) * hi_f(gw[k])); sm[p] += lo_f(pr) + hi_f(pr); }
        }
    }
#pragma unroll
    for (int p = 0; p < 8; ++p) {
        float s = sm[p];
        s += __shfl_xor(s, 1); s += __shfl_xor(s, 2); s += __shfl_xor(s, 4);
        s = rbf(s);
        bst4(RW, sub == 0 ? (gi * 256 + p * 32 + (tid >> 3)) * 4 : OOB, WS_G, __float_as_uint(rbf(1.f / (1.f + __expf(-s)))));
    }
    VMWAIT(0);
    BAR();
    if (tid == 0) bst4(RW, (C_GF + gi) * 4, WS_CTL, ep);
}

constexpr int CV_UC = 4288, CV_KH = 4 * CV_UC, CV_KL = CV_KH + 7680, CV_WAVE = CV_KL + 7680;
constexpr int CV_Y = 4 * CV_WAVE, CV_TP = 2576 + 4096;
static_assert(CV_WAVE % 16 == 0 && CV_Y + 4 * CV_TP <= LDS_ROLE && 64 * 264 <= 4 * CV_TP, "conv lds");
typedef float f32x2 __attribute__((ext_vector_type(2)));
__device__ __forceinline__ f32x2 fma2(f32x2 a, f32x2 b, f32x2 c) { return __builtin_elementwise_fma(a, b, c); }
__device__ __forceinline__ void conv_phase(const Args& a, int ci, LAS char* lds, rsrc_t RW, u32 ep) {
    const int tid = threadIdx.x, w = __builtin_amdgcn_readfirstlane(tid >> 6), l = tid & 63, cl = l & 15, g = l >> 4;
    const int d = ci * 4 + w;
    LAS char* wv = lds + w * CV_WAVE;
    LAS char* par = lds + CV_Y + w * CV_TP;
    u32x2 uv[8];
#pragma unroll
    for (int i = 0; i < 8; ++i) uv[i] = *(const u32x2*)(a.u + (size_t)(tid + 256 * i) * D + ci * 4);
    for (int i = l; i < CV_WAVE / 16; i += 64) *(LAS u32x4*)(wv + i * 16) = u32x4{0, 0, 0, 0};
    {
        const float dt = rbf(expf(bf2f(a.logdt[d])));
#pragma unroll
        for (int i = 0; i < 2; ++i) {
            const int n = l + 64 * i, o = d * NM + n;
            const float br = dt * bf2f(a.Bre[o]), bi = dt * bf2f(a.Bim[o]), cr = bf2f(a.Cre[o]), cim = bf2f(a.Cim[o]);
            const float li = dt * bf2f(a.Aim[o]), c1 = 0.15915494309189535f, c2 = 6.420638317453e-09f;
            const float lih = li * c1, lil = __builtin_fmaf(li, c2, __builtin_fmaf(li, c1, -lih));
            *(LAS f32x4*)(par + n * 32) = f32x4{dt * bf2f(a.Are[o]) * 1.4426950408889634f, lih, lil, 0.f};
            *(LAS f32x2*)(par + n * 32 + 16) = f32x2{cr * br - cim * bi, cr * bi + cim * br};
        }
    }
    BAR();
#pragma unroll
    for (int i = 0; i < 8; ++i) {
        const int p = 2064 - (tid + 256 * i);
#pragma unroll
        for (int c = 0; c < 4; ++c) {
            const u16 v = (u16)(c & 1 ? uv[i][c >> 1] >> 16 : uv[i][c >> 1]);
#pragma unroll
            for (int k = 0; k < 4; ++k) *(LAS u16*)(lds + c * CV_WAVE + k * CV_UC + (p + k) * 2) = v;
        }
    }
    {
        f32x4 ka[4] = {{0, 0, 0, 0}, {0, 0, 0, 0}, {0, 0, 0, 0}, {0, 0, 0, 0}}, kb[4] = {{0, 0, 0, 0}, {0, 0, 0, 0}, {0, 0, 0, 0}, {0, 0, 0, 0}};
        float last = 0.f;
        const f32x2 tau = {(float)cl, 16.f * cl};
#pragma unroll 2
        for (int t = 0; t < 32; ++t) {
            const f32x4 pr = *(LAS f32x4*)(par + (4 * t + g) * 32);
            const f32x2 cb = *(LAS f32x2*)(par + (4 * t + g) * 32 + 16);
            const f32x2 ar = tau * pr[0], rv = tau * pr[1];
            const f32x2 nn = {__builtin_rintf(rv[0]), __builtin_rintf(rv[1])};
            const f32x2 fr = fma2(tau, f32x2{pr[2], pr[2]}, fma2(tau, f32x2{pr[1], pr[1]}, -nn));
            const f32x2 mag = {__builtin_amdgcn_exp2f(ar[0]), __builtin_amdgcn_exp2f(ar[1])};
            f32x2 er = mag * f32x2{__builtin_amdgcn_cosf(fr[0]), __builtin_amdgcn_cosf(fr[1])};
            f32x2 ei = mag * f32x2{__builtin_amdgcn_sinf(fr[0]), __builtin_amdgcn_sinf(fr[1])};
#pragma unroll
            for (int e = 0; e < 4; ++e) {
                float xr = __builtin_fmaf(cb[0], er[1], -(cb[1] * ei[1])), nxi = __builtin_fmaf(-cb[0], ei[1], -(cb[1] * er[1]));
                asm volatile("" : "+v"(xr), "+v"(nxi));
                ka[e] = __builtin_amdgcn_mfma_f32_16x16x4f32(xr, er[0], ka[e], 0, 0, 0);
                kb[e] = __builtin_amdgcn_mfma_f32_16x16x4f32(nxi, ei[0], kb[e], 0, 0, 0);
                const f32x2 t2 = fma2(-ei, ei, er * er); ei = (er + er) * ei; er = t2;
            }
            last += __builtin_fmaf(cb[0], er[1], -(cb[1] * ei[1]));
        }
#pragma unroll
        for (int e = 0; e < 4; ++e) ka[e] += kb[e];
        last = swapadd16(last, last); last = swapadd32(last, last);
        LAS float* tab = (LAS float*)(par + 4096);
#pragma unroll
        for (int r = 0; r < 4; ++r) {
            tab[16 * (4 * g + r) + cl] = ka[0][r];
            if (g >= 2) {
#pragma unroll
                for (int e = 1; e < 4; ++e) tab[128 * e + 16 * (4 * g + r) + cl] = ka[e][r];
            }
        }
        if (l == 8) tab[640] = last;
        for (int i = 0; i < 16; ++i) {
            const int l0 = (i * 64 + l) * 2;
            float kv[2];
#pragma unroll
            for (int k = 0; k < 2; ++k) {
                const u32 rb = __float_as_uint(rbf((float)(l0 + k)));
                const int tau = (int)__uint_as_float(rb);
                const int e = (int)(rb >> 23) - 134;
                kv[k] = tab[tau < 256 ? tau : 128 * e + (tau >> e)];
            }
            const u32 hi2 = pk2(kv[0], kv[1]);
            const u32 lo2 = pk2(kv[0] - lo_f(hi2), kv[1] - hi_f(hi2));
            const int off = ((l0 >> 5) + 32) * 80 + (l0 & 31) * 2;
            *(LAS u32*)(wv + CV_KH + off) = hi2; *(LAS u32*)(wv + CV_KL + off) = lo2;
        }
    }
    BAR();
    f32x16 y0, y1;
#pragma unroll
    for (int r = 0; r < 16; ++r) { y0[r] = 0.f; y1[r] = 0.f; }
    {
        const int j = l & 31, hh = l >> 5;
        const LAS char* ap = wv + (j & 3) * CV_UC + 2 * (2080 - j + (j & 3) + 8 * hh);
        const LAS char* bp = wv + CV_KH + (j + 32) * 80 + 16 * hh;
        const LAS char* ap2 = ap + 8; asm volatile("" : "+v"(ap2));
        auto lda = [&](int t) { const u32x2 p0 = *(const LAS u32x2*)(ap - 32 * t), p1 = *(const LAS u32x2*)(ap2 - 32 * t); return u32x4{p0[0], p0[1], p1[0], p1[1]}; };
        auto ldb = [&](int t, int off) { return *(const LAS u32x4*)(bp - (t >> 1) * 80 + ((t & 1) ? 0 : 32) + off); };
        u32x4 A = lda(0), b1h = ldb(0, 2560), b1l = ldb(0, 2560 + 7680), b0h = ldb(0, 0), b0l = ldb(0, 7680);
#pragma unroll 4
        for (int t = 0; t < 64; ++t) {
            const u32x4 An = lda(t + 1), n1h = ldb(t + 1, 2560), n1l = ldb(t + 1, 2560 + 7680), n0h = ldb(t + 1, 0), n0l = ldb(t + 1, 7680);
            SB();
            y1 = mf3232(A, b1h, y1); y0 = mf3232(A, b0h, y0); y1 = mf3232(A, b1l, y1); y0 = mf3232(A, b0l, y0);
            SB();
            A = An; b1h = n1h; b1l = n1l; b0h = n0h; b0l = n0l;
        }
        u32x4 A2 = lda(65), c1h = ldb(65, 2560), c1l = ldb(65, 2560 + 7680);
#pragma unroll 2
        for (int t = 64; t < 128; t += 2) {
            const int t2 = t + 2 < 128 ? t + 2 : 126;
            const u32x4 An = lda(t2), n1h = ldb(t2, 2560), n1l = ldb(t2, 2560 + 7680), Bn = lda(t2 + 1), m1h = ldb(t2 + 1, 2560), m1l = ldb(t2 + 1, 2560 + 7680);
            SB();
            y1 = mf3232(A, b1h, y1); y1 = mf3232(A, b1l, y1); y1 = mf3232(A2, c1h, y1); y1 = mf3232(A2, c1l, y1);
            SB();
            A = An; b1h = n1h; b1l = n1l; A2 = Bn; c1h = m1h; c1l = m1l;
        }
#pragma unroll
        for (int r = 0; r < 16; ++r) {
            const int jj = (r & 3) + 8 * (r >> 2) + 4 * hh;
            const u32 pr = pk2(y0[r], y1[r]);
            *(LAS u16*)(lds + CV_Y + j * 264 + jj * 8 + w * 2) = (u16)pr;
            *(LAS u16*)(lds + CV_Y + (j + 32) * 264 + jj * 8 + w * 2) = (u16)(pr >> 16);
        }
    }
    BAR();
    {
        const u32x2 dk = *(const u32x2*)(a.Dskip + ci * 4);
#pragma unroll
        for (int i = 0; i < 8; ++i) {
            const int t = tid + 256 * i;
            const u32x2 y = *(const LAS u32x2*)(lds + CV_Y + (t >> 5) * 264 + (t & 31) * 8);
            u32x2 o;
#pragma unroll
            for (int k = 0; k < 2; ++k) {
                const u32 sk = pk2(lo_f(uv[i][k]) * lo_f(dk[k]), hi_f(uv[i][k]) * hi_f(dk[k]));
                o[k] = pk2(lo_f(y[k]) + lo_f(sk), hi_f(y[k]) + hi_f(sk));
            }
            bst8(RW, t * (D * 2), WS_YA + ci * 8, o);
        }
    }
    VMWAIT(0);
    BAR();
    if (tid == 0) bst4(RW, (C_CF + ci) * 4, WS_CTL, ep);
}

__device__ __forceinline__ void attn_phase(const Args& a, int h, int s0, int s1, int ds, rsrc_t RW, u32 ep) {
    const int tid = threadIdx.x, l = tid & 63, cl = l & 15, g = l >> 4;
    gu32* ctl = (gu32*)(a.ws + WS_CTL);
    const u32 l16 = l * 16;
    const u32 kmo = g * 128 + (4 * (cl >> 3) + (cl & 3)) * 16;
    const u32 vmo = (g & 1) ? OOB : cl * 32 + (g >> 1) * 8;
    const float scale = 0.08838834764831845f, l2e = 1.4426950408889634f, NEG = -998244352.f;
    bool dead = false;
    u32 nf = ep - 1;
    u32 pend = OOB;
    for (int s = s0; s < s1; s += ds) {
        const u32 sh = (u32)(s * H + h);
        const bool rdy = !__any(nf - ep - (l < 2 ? 0u : (u32)(s + 1)) > (l < 2 ? 0u : (u32)(63 - s)));
        if (!rdy) {
            if (pend != OOB) { VMWAIT(0); bst4(RW, l == 0 ? pend : OOB, WS_CTL, ep); pend = OOB; }
            wait_flags(RW, l == 0 ? (C_KVF + sh) * 4 : l == 1 ? (C_QF + sh) * 4 : OOB, ep, 0, ctl, dead);
        }
        u32x4 qf[2][4], kx[2][4], vx[8];
#pragma unroll
        for (int i = 0; i < 8; ++i) {
            qf[i >> 2][i & 3] = bld16(RW, l16 + (i & 3) * 1024, WS_QXF + sh * 8192 + (i >> 2) * 4096);
            kx[i >> 2][i & 3] = bld16(RW, l16 + (i & 3) * 1024, WS_KXF + sh * 8192 + (i >> 2) * 4096);
        }
#pragma unroll
        for (int i = 0; i < 8; ++i) vx[i] = bld16(RW, l16 + (i & 3) * 1024, WS_VXF + sh * 8192 + (i >> 2) * 4096);
        if (!rdy) wait_flags(RW, l == 0 ? (C_KMF + h * 32) * 4 : OOB, ep, s + 1, ctl, dead);
        u32x4 km[4]; u32x2 vm[8];
#pragma unroll
        for (int kk = 0; kk < 4; ++kk) km[kk] = bld16(RW, kmo + kk * 512, WS_KM + sh * 2048);
#pragma unroll
        for (int cc = 0; cc < 8; ++cc) vm[cc] = bld8(RW, vmo, WS_VMT + sh * 2048 + (cc >> 1) * 512 + (cc & 1) * 16);
        nf = bld4(RW, l == 0 ? (C_KVF + sh + ds * 32) * 4 : l == 1 ? (C_QF + sh + ds * 32) * 4 : (C_KMF + h * 32) * 4, 0);
        FENCE();
        if (pend != OOB) { VMWAIT(24); bst4(RW, l == 0 ? pend : OOB, WS_CTL, ep); }
        f32x4 sx00 = {0, 0, 0, 0}, sx10 = {0, 0, 0, 0}, sx11 = {0, 0, 0, 0};
#pragma unroll
        for (int kk = 0; kk < 4; ++kk) { sx00 = mf32(kx[0][kk], qf[0][kk], sx00); sx10 = mf32(kx[0][kk], qf[1][kk], sx10); sx11 = mf32(kx[1][kk], qf[1][kk], sx11); }
        f32x4 sm[2] = {{0, 0, 0, 0}, {0, 0, 0, 0}};
#pragma unroll
        for (int kk = 0; kk < 4; ++kk) { sm[0] = mf32(km[kk], qf[0][kk], sm[0]); sm[1] = mf32(km[kk], qf[1][kk], sm[1]); }
        u32x4 px[2]; u32x2 pm[2];
#pragma unroll
        for (int qt = 0; qt < 2; ++qt) {
            float v[12];
#pragma unroll
            for (int r = 0; r < 4; ++r) {
                v[r] = rbf(rbf(sm[qt][r]) * scale);
                const float dg = rbf(rbf(qt ? sx11[r] : sx00[r]) * scale);
                const float dm = (4 * g + r <= cl) ? dg : NEG;
                v[4 + r] = qt ? rbf(rbf(sx10[r]) * scale) : dm;
                v[8 + r] = qt ? dm : NEG;
            }
            float mx = v[0];
#pragma unroll
            for (int i = 1; i < 12; ++i) mx = fmaxf(mx, v[i]);
            mx = xmax16(mx); mx = xmax32(mx);
            float s0 = 0.f, s1 = 0.f;
#pragma unroll
            for (int i = 0; i < 12; ++i) { v[i] = __builtin_amdgcn_exp2f((v[i] - mx) * l2e); if (i < 4) s0 += v[i]; else s1 += v[i]; }
            float sum = s1 + ((g & 1) ? 0.f : s0);
            sum = swapadd16(sum, sum); sum = swapadd32(sum, sum);
            const float inv = 1.f / sum;
            px[qt] = u32x4{pk2(v[4] * inv, v[5] * inv), pk2(v[6] * inv, v[7] * inv), pk2(v[8] * inv, v[9] * inv), pk2(v[10] * inv, v[11] * inv)};
            pm[qt] = (g & 1) ? u32x2{0, 0} : u32x2{pk2(v[0] * inv, v[1] * inv), pk2(v[2] * inv, v[3] * inv)};
        }
#pragma unroll
        for (int qt = 0; qt < 2; ++qt)
#pragma unroll
            for (int wp = 0; wp < 4; ++wp) {
                f32x4 oA = mf32(vx[2 * wp], px[qt], f32x4{0, 0, 0, 0}), oB = mf32(vx[2 * wp + 1], px[qt], f32x4{0, 0, 0, 0});
                oA = mf16(vm[2 * wp], pm[qt], oA); oB = mf16(vm[2 * wp + 1], pm[qt], oB);
                bst16(RW, l16, WS_OXF + (u32)((s * 2 + qt) * 128 + h * 4 + wp) * 1024,
                      u32x4{pk2(oA[0], oB[0]), pk2(oA[1], oB[1]), pk2(oA[2], oB[2]), pk2(oA[3], oB[3])});
            }
        FENCE();
        pend = WS_OF + sh * 128;
    }
    VMWAIT(0);
    bst4(RW, l == 0 ? pend : OOB, WS_CTL, ep);
}

constexpr int NSET = 8;
__device__ __forceinline__ void oproj_phase(const Args& a, int c48, int set, LAS char* lds, rsrc_t RW, u32 ep) {
    const int tid = threadIdx.x, w = __builtin_amdgcn_readfirstlane(tid >> 6), l = tid & 63, cl = l & 15, g = l >> 4;
    gu32* ctl = (gu32*)(a.ws + WS_CTL);
    u32x4 wf[3][32];
    {
        LAS char* st = lds + w * 32768;
        const int q = cl >> 2;
        const int rq = 16 * (q & 1) + 4 * g + (q >> 1);
#pragma unroll
        for (int rnd = 0; rnd < 4; ++rnd) {
#pragma unroll
            for (int i = 0; i < 32; ++i)
                dma16(a.Wo + (size_t)(w * 1024 + rnd * 256 + i * 8 + (l >> 3)) * D + c48 * 48 + min(l & 7, 5) * 8, st + i * 1024);
            VMWAIT(0);
#pragma unroll
            for (int k = 0; k < 8; ++k)
#pragma unroll
                for (int ct = 0; ct < 3; ++ct) {
                    const LAS char* p = st + (k * 32 + rq) * 128 + ct * 32 + (cl & 3) * 8;
                    const u32x2 x = trread(p), y = trread(p + 2 * 128);
                    wf[ct][rnd * 8 + k] = (rnd * 8 + k < 20) ? toA4(u32x4{x[0], x[1], y[0], y[1]}) : toV4(u32x4{x[0], x[1], y[0], y[1]});
                }
            asm volatile("s_waitcnt lgkmcnt(0)" ::: "memory");
            SB();
        }
    }
    bool dead = false;
    wait_flags(RW, l < 12 ? (C_CF + c48 * 12 + l) * 4 : l < 20 ? (C_GF + l - 12) * 4 : OOB, ep, 0, ctl, dead);
    BAR();
    const u32 l16 = l * 16;
    int par = 0;
    bool pre = false;
    u32x4 o[16];
    for (int it = set; it < 2 * NSEG; it += NSET) {
        const int s = it >> 1;
        const int t = it * 16 + cl;
        const u32 ob = UNI(WS_OXF + (u32)(it * 128 + w * 32) * 1024);
        if (!pre) {
            wait_flags(RW, l < 8 ? WS_OF + (s * 32 + w * 8 + l) * 128 : OOB, ep, 0, ctl, dead);
#pragma unroll
            for (int k = 0; k < 16; ++k) o[k] = bld16(RW, l16 + (k & 3) * 1024, ob + (k >> 2) * 4096);
        }
        const u32 nf = bld4(RW, WS_OF + ((s + NSET / 2) * 32 + w * 8 + (l & 7)) * 128, 0);
        u32x2 ya; float gv;
        if (w < 3) { ya = bld8(RW, t * (D * 2) + g * 8, WS_YA + c48 * 96 + w * 32); gv = __uint_as_float(bld4(RW, t * 4, WS_G)); }
        f32x4 c0 = {0, 0, 0, 0}, c1 = {0, 0, 0, 0}, c2 = {0, 0, 0, 0};
        u32 nb = OOB;
#pragma unroll
        for (int k = 0; k < 32; ++k) {
            if (k == 16) { pre = it + NSET < 2 * NSEG && !__any(nf != ep); nb = pre ? ob + NSET * 131072 : OOB; FENCE(); }
            const u32x4 ok = o[k & 15];
            c0 = mf32(wf[0][k], ok, c0); c1 = mf32(wf[1][k], ok, c1); c2 = mf32(wf[2][k], ok, c2);
            SB();
            if (k < 16) o[k] = bld16(RW, l16 + (k & 3) * 1024, ob + (4 + (k >> 2)) * 4096);
            else o[k & 15] = bld16(RW, l16 + (k & 3) * 1024, nb + ((k & 15) >> 2) * 4096);
            SB();
        }
        LAS char* red = lds + 131072 + par * 12288;
        *(LAS f32x4*)(red + w * 3072 + l16) = c0; *(LAS f32x4*)(red + w * 3072 + 1024 + l16) = c1; *(LAS f32x4*)(red + w * 3072 + 2048 + l16) = c2;
        BAR();
        if (w < 3) {
            const f32x4 r0 = *(LAS f32x4*)(red + w * 1024 + l16), r1 = *(LAS f32x4*)(red + 3072 + w * 1024 + l16),
                        r2 = *(LAS f32x4*)(red + 6144 + w * 1024 + l16), r3 = *(LAS f32x4*)(red + 9216 + w * 1024 + l16);
            const f32x4 b = (r0 + r1) + (r2 + r3);
            const float og = rbf(1.f - gv);
            u32x2 res;
#pragma unroll
            for (int k = 0; k < 2; ++k) {
                const u32 bb = pk2(b[2 * k], b[2 * k + 1]);
                const u32 ga = pk2(gv * lo_f(ya[k]), gv * hi_f(ya[k])), gb = pk2(og * lo_f(bb), og * hi_f(bb));
                res[k] = pk2(lo_f(ga) + lo_f(gb), hi_f(ga) + hi_f(gb));
            }
            *(u32x2*)(a.out + (size_t)t * D + c48 * 48 + w * 16 + g * 4) = res;
        }
        par ^= 1;
    }
}

__device__ __forceinline__ void worker_role(const Args& a, int wid, LAS char* lds, u32 ep) {
    const rsrc_t RW = __builtin_amdgcn_make_buffer_rsrc(a.ws, 0, 0x7fffffff, 0x00020000);
    if (wid == 151) calibrate(a, lds, ep);
    if (wid < 144) { qkv_phase(a, wid, lds, RW, ep); conv_phase(a, wid, lds, RW, ep); }
    else {
        gate_phase(a, wid - 144, RW, ep);
    }
    if (wid < 96) oproj_phase(a, (wid >> 2) % 12, (wid & 3) * 2 + (wid >> 2) / 12, lds, RW, ep);
    else if (wid < 128 || wid >= 144) {
        const int wv = __builtin_amdgcn_readfirstlane(threadIdx.x >> 6);
        const bool early = wid >= 144;
        attn_phase(a, early ? (wid - 144) * 4 + wv : wid - 96, early ? 0 : SE + wv, early ? SE : NSEG, early ? 1 : 4, RW, ep);
    }
    if (wid == 0 && threadIdx.x == 0) __hip_atomic_store((gu32*)(a.ws + WS_CTL) + C_EPOCH, ep, RLX_AGENT);
}

__global__ void __launch_bounds__(256, 1) mega(Args a) {
    extern __shared__ __attribute__((aligned(16))) char smem[];
    LAS char* lds = (LAS char*)smem;
    gu32* ctl = (gu32*)(a.ws + WS_CTL);
    if (threadIdx.x >= 64) {
        const int wv = __builtin_amdgcn_readfirstlane(threadIdx.x >> 6);
        const u16* p = a.Wo + (size_t)(blockIdx.x * 3 + wv - 1) * 3072 + (threadIdx.x & 63) * 8;
#pragma unroll
        for (int i = 0; i < 6; ++i) dma16(p + i * 512, lds + wv * 1024);
    }
    if (threadIdx.x == 0) {
        const u32 e = __hip_atomic_load(ctl + C_EPOCH, RLX_AGENT) + 256u;
        const u32 x = (u32)__builtin_amdgcn_s_getreg(20 | (3 << 11)) & 7u, t0 = blockIdx.x >> 3;
        u32 slot = 256;
        for (u32 k = 0; k < 256; ++k) {
            const u32 c = (((x + (k >> 5)) & 7u) << 5) | ((t0 + k) & 31u);
            if (__hip_atomic_exchange(ctl + C_SLOT + c, e, RLX_AGENT) != e) { slot = c; break; }
        }
        *(LAS u32x2*)(lds + LDS_ROLE) = u32x2{slot, e};
    }
    __syncthreads();
    const int slot = UNI(*(LAS u32*)(lds + LDS_ROLE));
    const u32 ep = UNI(*(LAS u32*)(lds + LDS_ROLE + 4));
    const int x = slot >> 5, t = slot & 31;
    if (slot < 256) {
        if (x < 4 && t < 8) head_role(a, t * 4 + x, lds, ep);
        else if (x < 4 && t < 26) o_role(a, (18 * (x >> 1) + t - 8) * 2 + (x & 1), lds, ep);
        else worker_role(a, x >= 4 ? t * 4 + (x - 4) : 128 + (t - 26) * 4 + x, lds, ep);
    }
}

extern "C" void kernel_launch(void* const* d_in, const int* in_sizes, int n_in, void* d_out, int out_size, void* d_ws, size_t ws_size, hipStream_t stream) {
    Args a;
    a.u = (const u16*)d_in[0]; a.Are = (const u16*)d_in[1]; a.Aim = (const u16*)d_in[2]; a.Bre = (const u16*)d_in[3]; a.Bim = (const u16*)d_in[4];
    a.Cre = (const u16*)d_in[5]; a.Cim = (const u16*)d_in[6]; a.logdt = (const u16*)d_in[7]; a.Dskip = (const u16*)d_in[8]; a.mem0 = (const u16*)d_in[10];
    a.Wq = (const u16*)d_in[11]; a.Wk = (const u16*)d_in[12]; a.Wv = (const u16*)d_in[13]; a.Wo = (const u16*)d_in[14]; a.gw = (const u16*)d_in[16];
    a.out = (u16*)d_out; a.ws = (char*)d_ws;
    (void)hipFuncSetAttribute((const void*)mega, hipFuncAttributeMaxDynamicSharedMemorySize, LDS_TOTAL);
    mega<<<256, 256, LDS_TOTAL, stream>>>(a);
}
```
